```python
import torch

def _softmax(x, axis=-1):
    return torch.softmax(x, dim=axis)

def _gelu(x):
    return torch.nn.functional.gelu(x, approximate='tanh')

def _silu(x):
    return torch.nn.functional.silu(x)

def _sigmoid(x):
    return torch.sigmoid(x)

def _layernorm(x, axis=-1, eps=1e-05):
    m = x.mean(dim=axis, keepdim=True)
    v = x.var(dim=axis, keepdim=True, unbiased=False)
    return (x - m) / torch.sqrt(v + eps)

def _scatter_add(tgt, idx, src, axis=0):
    return tgt.clone().index_add_(axis, idx, src)

def _segment_sum(src, seg, n):
    out = torch.zeros((n,) + src.shape[1:], dtype=src.dtype)
    return out.index_add_(0, seg, src)

def _pad(x, pw):
    flat = tuple((v for pair in reversed(pw) for v in pair))
    return torch.nn.functional.pad(x, flat)
kernel_name = 'gated_mix__mha_block__mixer_gmlp__prompt'

def setup_inputs():

    _w = lambda a: (lambda t: t.to(torch.bfloat16) if t.is_floating_point() else t)(torch.as_tensor(a))

    def _A_setup():
        B, S, D, H = (1, 2048, 576, 32)
        return {'x': _w((lambda *s: torch.randn(*s, dtype=torch.bfloat16) * 0.05)(B, S, D)), 'wqkv': _w((lambda *s: torch.randn(*s, dtype=torch.bfloat16) * 1.6)(D, 3 * D)), 'wo': _w((lambda *s: torch.randn(*s, dtype=torch.bfloat16) * 0.1)(D, D))}
    _A_in = _A_setup()

    def _B_setup():
        B, S, D, F = (1, 2048, 576, 4608)
        Fh = F // 2
        return {'x': _w((lambda *s: torch.randn(*s, dtype=torch.bfloat16) * 0.05)(B, S, D)), 'w_in': _w((lambda *s: torch.randn(*s, dtype=torch.bfloat16) * 1.6)(D, 2 * Fh)), 'w_sp': _w(0.01 * (lambda *s: torch.randn(*s, dtype=torch.bfloat16) * 1.6)(S, S)), 'b_sp': _w(1.0 + 0.01 * (lambda *s: torch.randn(*s, dtype=torch.bfloat16) * 0.05)(S)), 'w_out': _w((lambda *s: torch.randn(*s, dtype=torch.bfloat16) * 0.05)(Fh, D))}
    _B_in = _B_setup()
    return {'A_x': _A_in['x'], 'A_wqkv': _A_in['wqkv'], 'A_wo': _A_in['wo'], 'B_x': _B_in['x'], 'B_w_in': _B_in['w_in'], 'B_w_sp': _B_in['w_sp'], 'B_b_sp': _B_in['b_sp'], 'B_w_out': _B_in['w_out'], 'gate_w': _w((lambda *s: torch.randn(*s, dtype=torch.bfloat16) * 0.05)(576) * 16.0)}

def reference(A_x, A_wqkv, A_wo, B_x, B_w_in, B_w_sp, B_b_sp, B_w_out, gate_w):

    def _A_ref(x, wqkv, wo):
        B, S, D = x.shape
        H = 32
        Dh = D // H
        qkv = (lambda x, *d: x.permute(*d))((x @ wqkv).reshape(B, S, 3, H, Dh), 2, 0, 3, 1, 4)
        q, k, v = (qkv[0], qkv[1], qkv[2])
        scores = torch.einsum('bhqd,bhkd->bhqk', q, k) * Dh ** (-0.5)
        p = _softmax(scores, axis=-1)
        o = torch.einsum('bhqk,bhkd->bhqd', p, v)
        return (lambda x, *d: x.permute(*d))(o, 0, 2, 1, 3).reshape(B, S, D) @ wo

    def _B_ref(x, w_in, w_sp, b_sp, w_out):
        B, S, D = x.shape
        F = w_in.shape[-1]
        Fh = F // 2
        z = _gelu(torch.einsum('bsd,df->bsf', x, w_in))
        z1 = z[..., :Fh].clone()
        z2 = _layernorm(z[..., Fh:], axis=-1)
        g = torch.einsum('ts,bsf->btf', w_sp, z2) + b_sp[None, :, None]
        return torch.einsum('bsf,fd->bsd', z1 * g, w_out)
    x = A_x
    g = _sigmoid((x * gate_w).sum(-1, keepdims=True))
    a = _A_ref(x, A_wqkv, A_wo)
    b = _B_ref(x, B_w_in, B_w_sp, B_b_sp, B_w_out)
    return g * a + (1 - g) * b
```

```cpp
#include <hip/hip_runtime.h>
#include <stdint.h>

typedef unsigned short u16;
typedef short bf16x8 __attribute__((ext_vector_type(8)));
typedef short s16x4 __attribute__((ext_vector_type(4)));
typedef float f32x2 __attribute__((ext_vector_type(2)));
typedef float f32x4 __attribute__((ext_vector_type(4)));
typedef float f32x16 __attribute__((ext_vector_type(16)));
typedef unsigned u32x2 __attribute__((ext_vector_type(2)));
typedef unsigned u32x4 __attribute__((ext_vector_type(4)));
typedef __bf16 bf16x2_t __attribute__((ext_vector_type(2)));
#define LAS __attribute__((address_space(3)))
#define GAS __attribute__((address_space(1)))
typedef LAS char* lptr;
#define DI __device__ __forceinline__

constexpr int SEQ = 2048, DM = 576, FH = 2304, NH = 32;

constexpr size_t WS_Z2N = 0;
constexpr size_t WS_Z1 = WS_Z2N + 9437184;
constexpr size_t WS_U = WS_Z1 + 9437184;
constexpr size_t WS_QF1 = WS_U + 9437184;
constexpr size_t WS_Q2 = WS_QF1 + 2097152;
constexpr size_t WS_KF1 = WS_Q2 + 262144;
constexpr size_t WS_K2X = WS_KF1 + 2097152;
constexpr size_t WS_VT = WS_K2X + 1048576;
constexpr size_t WS_KMAX = WS_VT + 2359296;
constexpr size_t WS_O = WS_KMAX + 8192;
constexpr size_t WS_GATE = WS_O + 2359296;
constexpr size_t WS_XBUF = WS_GATE + 8192;
constexpr size_t WS_FLAG = WS_XBUF + 262144;
constexpr size_t WS_NONCE = WS_FLAG + 8192;
constexpr size_t WS_END = WS_NONCE + 2048;

struct Params {
    const u16* ax; const u16* wqkv; const u16* wo; const u16* bx; const u16* win; const u16* wsp;
    const u16* bsp; const u16* wout; const u16* gw; u16* out; char* ws;
};

DI unsigned pk2(float lo, float hi) { f32x2 v = {lo, hi}; return __builtin_bit_cast(unsigned, __builtin_convertvector(v, bf16x2_t)); }
DI float bflo(unsigned u) { return __uint_as_float(u << 16); }
DI float bfhi(unsigned u) { return __uint_as_float(u & 0xffff0000u); }
DI float red16(float x) { auto r = __builtin_amdgcn_permlane16_swap(__float_as_uint(x), __float_as_uint(x), false, false); return __uint_as_float(r[0]) + __uint_as_float(r[1]); }
DI float red32(float x) { auto r = __builtin_amdgcn_permlane32_swap(__float_as_uint(x), __float_as_uint(x), false, false); return __uint_as_float(r[0]) + __uint_as_float(r[1]); }
DI float max32(float x) { auto r = __builtin_amdgcn_permlane32_swap(__float_as_uint(x), __float_as_uint(x), false, false); return fmaxf(__uint_as_float(r[0]), __uint_as_float(r[1])); }
DI float gelu_tanh(float x) {
    constexpr float C0 = -2.0f * 0.7978845608028654f * 1.4426950408889634f, C1 = C0 * 0.044715f;
    const float t = x * x, p = __builtin_fmaf(t, C1, C0), e = __builtin_amdgcn_exp2f(x * p);
    return x * __builtin_amdgcn_rcpf(1.0f + e);
}
DI void glds(const void* sbase, unsigned voff, unsigned lds_addr) {
    unsigned keep;
    asm volatile("s_mov_b32 %0, m0\n\ts_mov_b32 m0, %3\n\ts_nop 0\n\tglobal_load_lds_dwordx4 %1, %2\n\ts_mov_b32 m0, %0"
                 : "=&s"(keep) : "v"(voff), "s"(sbase), "s"(lds_addr) : "memory");
}
DI void glds_m(const void* sbase, unsigned voff, unsigned lds_addr, unsigned long long mask) {
    unsigned keep; unsigned long long ex;
    asm volatile("s_mov_b32 %0, m0\n\ts_mov_b64 %1, exec\n\ts_mov_b32 m0, %4\n\ts_mov_b64 exec, %5\n\tglobal_load_lds_dwordx4 %2, %3\n\ts_mov_b64 exec, %1\n\ts_mov_b32 m0, %0"
                 : "=&s"(keep), "=&s"(ex) : "v"(voff), "s"(sbase), "s"(lds_addr), "s"(mask) : "memory");
}
#define WAIT_BAR0() asm volatile("s_waitcnt vmcnt(0) lgkmcnt(0)\n\ts_barrier" ::: "memory")
#define LGKM0() asm volatile("s_waitcnt lgkmcnt(0)" ::: "memory")
DI s16x4 trread(lptr p) { return __builtin_amdgcn_ds_read_tr16_b64_v4i16((LAS s16x4*)p); }
#define MFMA16(a, b, c) __builtin_amdgcn_mfma_f32_16x16x32_bf16((a), (b), (c), 0, 0, 0)
#define MFMA32(a, b, c) __builtin_amdgcn_mfma_f32_32x32x16_bf16((a), (b), (c), 0, 0, 0)

DI int rho2k(int rho) { const int r = rho & 31; return (rho & 32) + 8 * ((r >> 2) & 3) + 4 * (r >> 4) + (r & 3); }

constexpr int LDS_BYTES = 81920;
constexpr int A_SLOT = 9216, B_RING = 4 * A_SLOT, B_SLOT = 16384, PF = 3;
#define SB() __builtin_amdgcn_sched_barrier(0)
#define PIN(x) asm volatile("" : "+v"(x))
template <int NF> struct ACfg {
    static constexpr int P = NF * 32, NCH = NF * 2, TOT = 32 * NCH, FULL = TOT / 256, REM = (TOT - FULL * 256) / 4, NA = FULL + 1;
    static constexpr unsigned long long MASK = (1ull << REM) - 1ull;
};
#define TL(kid, n) do {} while (0)
#define TLC(kid, n) do {} while (0)
#define ST(acc, code) do { code; } while (0)
template <int N> DI void wait_bar() { asm volatile("s_waitcnt vmcnt(%0) lgkmcnt(0)\n\ts_barrier" :: "n"(N) : "memory"); }
DI void m0_set(unsigned base, int imm) { asm volatile("s_add_u32 m0, %0, %1" :: "s"(base), "i"(imm) : "scc", "memory"); }
DI void dma(const void* sbase, unsigned voff) { asm volatile("global_load_lds_dwordx4 %0, %1" :: "v"(voff), "s"(sbase) : "memory"); }
DI void dma_m(const void* sbase, unsigned voff, unsigned long long mask) {
    asm volatile("s_mov_b64 exec, %2\n\tglobal_load_lds_dwordx4 %0, %1\n\ts_mov_b64 exec, -1" :: "v"(voff), "s"(sbase), "s"(mask) : "memory");
}

template <int NF> DI int a_chunk_id(int n, int w, int lane) { return n < ACfg<NF>::FULL ? (n * 4 + w) * 64 + lane : ACfg<NF>::FULL * 256 + w * ACfg<NF>::REM + (lane < ACfg<NF>::REM ? lane : 0); }
template <int NF> DI void a_offsets_rowmajor(unsigned (&off)[ACfg<NF>::NA], int w, int lane, unsigned ldb) {
#pragma unroll
    for (int n = 0; n < ACfg<NF>::NA; ++n) {
        const int id = a_chunk_id<NF>(n, w, lane), rho = id / ACfg<NF>::NCH, c = id - rho * ACfg<NF>::NCH;
        off[n] = (unsigned)rho2k(rho) * ldb + (unsigned)c * 16u;
    }
}
template <int NF> DI void a_offsets_linear(unsigned (&off)[ACfg<NF>::NA], int w, int lane) {
#pragma unroll
    for (int n = 0; n < ACfg<NF>::NA; ++n) off[n] = (unsigned)a_chunk_id<NF>(n, w, lane) * 16u;
}
template <int NI> DI void b_offsets_rowmajor(unsigned (&off)[NI], int rw, int lane, unsigned ldb) {
#pragma unroll
    for (int n = 0; n < NI; ++n) { const int row = rw + 8 * n + (lane >> 3); off[n] = (unsigned)row * ldb + (unsigned)(((lane & 7) ^ (row & 7)) * 16); }
}
template <int NF, bool ATR> struct Gemm {
    static constexpr int P = ACfg<NF>::P, NA = ACfg<NF>::NA, FULL = ACfg<NF>::FULL;
    unsigned aoff[NA], boff[4];
    lptr pa, pb0, pb1;
    unsigned dA, dAm, dB;
    unsigned sa, sb;
    bf16x8 af[PF], bc[2];
    unsigned long long twait = 0;
    DI void init(lptr lds, unsigned lds0, int w, int lane) {
        const int g = lane >> 4, q = (lane & 15) >> 2, p = lane & 3;
        pa = ATR ? lds + (4 * g + q) * P + 8 * p : lds + lane * 16;
        pb0 = lds + B_RING + (32 * w + (lane & 15)) * 128 + ((g ^ (lane & 7)) * 16);
        pb1 = lds + B_RING + (32 * w + (lane & 15)) * 128 + (((4 + g) ^ (lane & 7)) * 16);
        dA = lds0 + w * 1024; dAm = lds0 + FULL * 4096 + w * ACfg<NF>::REM * 16; dB = lds0 + B_RING + w * 4096;
        sa = 0; sb = 0;
    }
    DI bf16x8 read_a(lptr p, int i) const {
        if constexpr (ATR) { const s16x4 lo = trread(p + 32 * i), hi = trread(p + 16 * P + 32 * i); return __builtin_shufflevector(lo, hi, 0, 1, 2, 3, 4, 5, 6, 7); }
        else return *(LAS bf16x8*)(p + 1024 * i);
    }
    DI void a_m0(int n, unsigned slot) const { if (n < FULL) m0_set(dA + slot, n * 4096); else m0_set(dAm + slot, 0); }
    DI void a_go(int n, const char* src) const { if (n < FULL) dma(src, aoff[n]); else dma_m(src, aoff[n], ACfg<NF>::MASK); }
    DI void issue_a(const char* src, unsigned slot) const {
#pragma unroll
        for (int n = 0; n < NA; ++n) { a_m0(n, slot); asm volatile("s_nop 0"); a_go(n, src); }
    }
    DI void issue_b(const char* src, unsigned slot) const {
#pragma unroll
        for (int n = 0; n < 4; ++n) { m0_set(dB + slot, n * 1024); asm volatile("s_nop 0"); dma(src, boff[n]); }
    }
    DI void prologue(const char* a0, const char* a1, const char* a2, const char* b0, const char* b1) {
        issue_b(b0, 0); issue_a(a0, 0); issue_a(a1, A_SLOT); issue_b(b1, B_SLOT); issue_a(a2, 2 * A_SLOT);
        wait_bar<2 * NA + 4>();
#pragma unroll
        for (int i = 0; i < PF; ++i) af[i] = read_a(pa, i);
        bc[0] = *(LAS bf16x8*)(pb0); bc[1] = *(LAS bf16x8*)(pb0 + 2048);
    }
    template <int NWAIT, bool DOB, bool DOA>
    DI void substep(f32x4 (&acc)[NF][2], unsigned sc, unsigned sn, lptr pbn, const char* srcB, unsigned slotB, const char* srcA, unsigned slotA) {
        ST(twait, wait_bar<NWAIT>());
        bf16x8 f[NF + PF], bn[2];
#pragma unroll
        for (int i = 0; i < PF; ++i) f[i] = af[i];
        constexpr int NB = DOB ? 4 : 0, ND = NB + (DOA ? NA : 0);
#pragma unroll
        for (int i = 0; i < NF; ++i) {
            const int d0 = (i * ND + NF - 1) / NF, d1 = ((i + 1) * ND + NF - 1) / NF;
            f[i + PF] = (i + PF < NF) ? read_a(pa + sc, i + PF) : read_a(pa + sn, i + PF - NF);
            if (i == 2) bn[0] = *(LAS bf16x8*)(pbn);
            if (i == 4) bn[1] = *(LAS bf16x8*)(pbn + 2048);
            if (d1 > d0) { if (d0 < NB) m0_set(dB + slotB, d0 * 1024); else a_m0(d0 - NB, slotA); }
            SB();
            acc[i][0] = MFMA16(f[i], bc[0], acc[i][0]);
            SB();
            if (d1 > d0) { if (d0 < NB) dma(srcB, boff[d0]); else a_go(d0 - NB, srcA); }
            if (d1 > d0 + 1) { if (d0 + 1 < NB) m0_set(dB + slotB, (d0 + 1) * 1024); else a_m0(d0 + 1 - NB, slotA); }
            SB();
            acc[i][1] = MFMA16(f[i], bc[1], acc[i][1]);
            SB();
            if (d1 > d0 + 1) { if (d0 + 1 < NB) dma(srcB, boff[d0 + 1]); else a_go(d0 + 1 - NB, srcA); }
        }
#pragma unroll
        for (int i = 0; i < PF; ++i) af[i] = f[NF + i];
        bc[0] = bn[0]; bc[1] = bn[1];
    }
    template <int MODE>
    DI void kstep(f32x4 (&acc)[NF][2], const char* aE, const char* bN, const char* aO) {
        const unsigned a1 = sa + A_SLOT >= 4 * A_SLOT ? sa + A_SLOT - 4 * A_SLOT : sa + A_SLOT;
        const unsigned a2 = a1 + A_SLOT >= 4 * A_SLOT ? a1 + A_SLOT - 4 * A_SLOT : a1 + A_SLOT;
        const unsigned a3 = a2 + A_SLOT >= 4 * A_SLOT ? a2 + A_SLOT - 4 * A_SLOT : a2 + A_SLOT;
        const unsigned sb1 = sb ^ B_SLOT;
        substep<MODE == 2 ? 0 : NA + 4, false, MODE != 2>(acc, sa, a1, pb1 + sb, nullptr, 0, aE, a3);
        substep<MODE == 2 ? 0 : NA, MODE == 0, MODE == 0>(acc, a1, a2, pb0 + sb1, bN, sb, aO, sa);
        sa = a2; sb = sb1;
    }
};
template <int NF, bool ATR, class Src>
DI void gemm_run(Gemm<NF, ATR>& G, f32x4 (&acc)[NF][2], const Src& S, int k0, int k1, int kt) {
    const int km = k1 < kt - 2 ? k1 : kt - 2;
    for (int k = k0; k < km; ++k) G.template kstep<0>(acc, S.a(2 * k + 3), S.b(k + 2), S.a(2 * k + 4));
    if (k1 == kt) {
        G.template kstep<1>(acc, S.a(2 * kt - 1), nullptr, nullptr);
        G.template kstep<2>(acc, nullptr, nullptr, nullptr);
    }
}
struct Src1 {
    const char* a0; size_t asub; const char* b0;
    DI const char* a(int s) const { return a0 + (size_t)s * asub; }
    DI const char* b(int k) const { return b0 + (size_t)k * 128; }
};
struct Src2 {
    const char* a0; const char* a1; size_t asub; const char* b0; int n;
    DI const char* a(int s) const { return s < 2 * n ? a0 + (size_t)s * asub : a1 + (size_t)(s - 2 * n) * asub; }
    DI const char* b(int k) const { return b0 + (size_t)(k < n ? k : k - n) * 128; }
};
template <int NF> DI void zero_acc(f32x4 (&acc)[NF][2]) {
#pragma unroll
    for (int i = 0; i < NF; ++i) { acc[i][0] = f32x4{0, 0, 0, 0}; acc[i][1] = f32x4{0, 0, 0, 0}; }
}

DI void l1_role_g(const Params& P, lptr lds, unsigned lds0, int idx, int w, int lane) {
    const int x = idx & 7, y = idx >> 3;
    const int r = (x >> 1) * 4 + (y >> 3), j = (x & 1) * 8 + (y & 7);
    const int g = lane >> 4;
    char* ws = P.ws;
    GAS unsigned long long* noncep = (GAS unsigned long long*)(ws + WS_NONCE) + r * 16;
    const unsigned long long tag = __hip_atomic_load(noncep, __ATOMIC_RELAXED, __HIP_MEMORY_SCOPE_AGENT) + 1ull;

    Gemm<9, true> G; G.init(lds, lds0, w, lane);
    a_offsets_rowmajor<9>(G.aoff, w, lane, 4608 * 2);
    b_offsets_rowmajor<4>(G.boff, 32 * w, lane, DM * 2);
    const Src2 S{(const char*)P.win + (size_t)(FH + 144 * j) * 2, (const char*)P.win + (size_t)(144 * j) * 2, (size_t)32 * 4608 * 2, (const char*)P.ax + (size_t)r * 128 * DM * 2, 9};
    G.prologue(S.a(0), S.a(1), S.a(2), S.b(0), S.b(1));
    TL(0, 1);
    {
        const int row = r * 128 + j * 8 + (w * 2 + (lane >> 5)), l32 = lane & 31;
        float s = 0.f;
#pragma unroll
        for (int n = 0; n < 3; ++n) {
            const int c = l32 + 32 * n;
            if (c < 72) {
                const u32x4 xv = *(const u32x4*)(P.ax + (size_t)row * DM + c * 8), gv = *(const u32x4*)(P.gw + c * 8);
#pragma unroll
                for (int e = 0; e < 4; ++e) s += bflo(xv[e]) * bflo(gv[e]) + bfhi(xv[e]) * bfhi(gv[e]);
            }
        }
#pragma unroll
        for (int o = 16; o >= 1; o >>= 1) s += __shfl_xor(s, o);
        if (l32 == 0) ((float*)(ws + WS_GATE))[row] = __builtin_amdgcn_rcpf(1.0f + __builtin_amdgcn_exp2f(-1.4426950408889634f * s));
    }

    f32x4 acc[9][2];
    zero_acc<9>(acc);
    gemm_run(G, acc, S, 0, 9, 18);
    TL(0, 2);

    unsigned zp[9][2][2];
    {
        float mean[2], m2[2];
#pragma unroll
        for (int tt = 0; tt < 2; ++tt) {
            float s = 0.f;
#pragma unroll
            for (int i = 0; i < 9; ++i)
#pragma unroll
                for (int e = 0; e < 4; ++e) { acc[i][tt][e] = gelu_tanh(acc[i][tt][e]); s += acc[i][tt][e]; }
            s = red32(red16(s));
            mean[tt] = s * (1.0f / 144.0f);
            float qq = 0.f;
#pragma unroll
            for (int i = 0; i < 9; ++i) {
#pragma unroll
                for (int e = 0; e < 4; ++e) { const float d = acc[i][tt][e] - mean[tt]; qq = __builtin_fmaf(d, d, qq); }
                zp[i][tt][0] = pk2(acc[i][tt][0], acc[i][tt][1]); zp[i][tt][1] = pk2(acc[i][tt][2], acc[i][tt][3]);
            }
            m2[tt] = red32(red16(qq));
        }
        GAS unsigned long long* xb64 = (GAS unsigned long long*)(ws + WS_XBUF) + (size_t)(r * 16 + j) * 128 + 32 * w;
        if (lane < 32) {
            const int tt = lane >> 4;
            const float mu = tt ? mean[1] : mean[0], mm = tt ? m2[1] : m2[0];
            __hip_atomic_store(xb64 + lane, ((unsigned long long)__float_as_uint(mm) << 32) | __float_as_uint(mu), __ATOMIC_RELAXED, __HIP_MEMORY_SCOPE_AGENT);
        }
        asm volatile("s_waitcnt vmcnt(0)" ::: "memory");
        if (lane == 0) __hip_atomic_store((GAS unsigned long long*)(ws + WS_FLAG) + (r * 4 + w) * 16 + j, tag, __ATOMIC_RELAXED, __HIP_MEMORY_SCOPE_AGENT);
    }
    TL(0, 3);
    zero_acc<9>(acc);
    gemm_run(G, acc, S, 9, 18, 18);
    TL(0, 4);
    {
        u32x4* z1p = (u32x4*)(ws + WS_Z1) + (size_t)idx * 9 * 256 + w * 64 + lane;
#pragma unroll
        for (int i = 0; i < 9; ++i) {
            u32x4 v;
            v[0] = pk2(gelu_tanh(acc[i][0][0]), gelu_tanh(acc[i][0][1])); v[1] = pk2(gelu_tanh(acc[i][0][2]), gelu_tanh(acc[i][0][3]));
            v[2] = pk2(gelu_tanh(acc[i][1][0]), gelu_tanh(acc[i][1][1])); v[3] = pk2(gelu_tanh(acc[i][1][2]), gelu_tanh(acc[i][1][3]));
            z1p[i * 256] = v;
        }
    }
    {
        GAS unsigned long long* fl = (GAS unsigned long long*)(ws + WS_FLAG) + (r * 4 + w) * 16 + (lane & 15);
        for (unsigned spins = 0; spins < (1u << 20); ++spins) {
            const unsigned long long v = __hip_atomic_load(fl, __ATOMIC_RELAXED, __HIP_MEMORY_SCOPE_AGENT);
            if (__all(v == tag)) break;
            __builtin_amdgcn_s_sleep(2);
        }
        __builtin_amdgcn_fence(__ATOMIC_ACQUIRE, "wavefront");
        if (j == 0 && w == 0 && lane == 0) __hip_atomic_store(noncep, tag, __ATOMIC_RELAXED, __HIP_MEMORY_SCOPE_AGENT);
    }
    TL(0, 5);
    float mu[2], rs[2];
    {
        GAS unsigned long long* xb64 = (GAS unsigned long long*)(ws + WS_XBUF) + (size_t)(r * 16 + (lane >> 5) * 8) * 128 + 32 * w + (lane & 31);
        float m[8], q2[8], sm = 0.f;
#pragma unroll
        for (int e = 0; e < 8; ++e) {
            const unsigned long long v = __hip_atomic_load(xb64 + e * 128, __ATOMIC_RELAXED, __HIP_MEMORY_SCOPE_AGENT);
            m[e] = __uint_as_float((unsigned)v); q2[e] = __uint_as_float((unsigned)(v >> 32)); sm += m[e];
        }
        const float mean = red32(sm) * (1.0f / 16.0f);
        float qq = 0.f;
#pragma unroll
        for (int e = 0; e < 8; ++e) { const float d = m[e] - mean; qq += q2[e] + 144.0f * d * d; }
        qq = red32(qq);
        const float rstd = 1.0f / sqrtf(qq * (1.0f / 2304.0f) + 1e-5f);
#pragma unroll
        for (int tt = 0; tt < 2; ++tt) { mu[tt] = __shfl(mean, 16 * tt + (lane & 15)); rs[tt] = __shfl(rstd, 16 * tt + (lane & 15)); }
    }
    WAIT_BAR0();
    {
        const lptr st = lds + w * 9216;
#pragma unroll
        for (int tt = 0; tt < 2; ++tt) {
            const int gp = 2 * tt + ((lane & 15) >> 3), h = (lane >> 2) & 1, ii = lane & 3;
            const lptr rowp = st + (16 * h + 4 * gp + ii) * 288 + 8 * g;
            const float a = rs[tt], b = -mu[tt] * rs[tt];
#pragma unroll
            for (int i = 0; i < 9; ++i) {
                u32x2 v;
                v[0] = pk2(__builtin_fmaf(bflo(zp[i][tt][0]), a, b), __builtin_fmaf(bfhi(zp[i][tt][0]), a, b));
                v[1] = pk2(__builtin_fmaf(bflo(zp[i][tt][1]), a, b), __builtin_fmaf(bfhi(zp[i][tt][1]), a, b));
                *(LAS u32x2*)(rowp + 32 * i) = v;
            }
        }
        LGKM0();
        const lptr rd = st + (4 * g + ((lane & 15) >> 2)) * 288 + 8 * (lane & 3);
        char* dst = ws + WS_Z2N + (size_t)(j * 64 + 4 * r + w) * 9216 + lane * 16;
#pragma unroll
        for (int i = 0; i < 9; ++i) {
            const s16x4 lo = trread(rd + 32 * i), hi = trread(rd + 16 * 288 + 32 * i);
            *(bf16x8*)(dst + i * 1024) = __builtin_shufflevector(lo, hi, 0, 1, 2, 3, 4, 5, 6, 7);
        }
    }
}

DI void l1_role_a(const Params& P, lptr lds, unsigned lds0, int idx, int w, int lane) {
    const int x = idx & 7, y = idx >> 3;
    const int r = (x >> 1) * 4 + (y >> 3), c = (x & 1) * 8 + (y & 7);
    const int g = lane >> 4;
    char* ws = P.ws;
    const int col0 = (108 * c) & ~7, shift = 108 * c - col0;
    Gemm<7, true> G; G.init(lds, lds0, w, lane);
    a_offsets_rowmajor<7>(G.aoff, w, lane, 1728 * 2);
    b_offsets_rowmajor<4>(G.boff, 32 * w, lane, DM * 2);
    const Src1 S{(const char*)P.wqkv + (size_t)col0 * 2, (size_t)32 * 1728 * 2, (const char*)P.ax + (size_t)r * 128 * DM * 2};
    G.prologue(S.a(0), S.a(1), S.a(2), S.b(0), S.b(1));
    TL(0, 1);
    f32x4 acc[7][2];
    zero_acc<7>(acc);
    gemm_run(G, acc, S, 0, 9, 9);
    TL(0, 2);
    WAIT_BAR0();
    constexpr int TP = 232;
    constexpr float QS = 0.23570226039551584f * 1.4426950408889634f;
    const lptr T = lds + w * (32 * TP);
#pragma unroll
    for (int i = 0; i < 7; ++i) {
        float sc[4];
#pragma unroll
        for (int e = 0; e < 4; ++e) sc[e] = (col0 + 16 * i + 4 * g + e < 576) ? QS : 1.0f;
#pragma unroll
        for (int tt = 0; tt < 2; ++tt) {
            u32x2 v; v[0] = pk2(acc[i][tt][0] * sc[0], acc[i][tt][1] * sc[1]); v[1] = pk2(acc[i][tt][2] * sc[2], acc[i][tt][3] * sc[3]);
            *(LAS u32x2*)(T + (16 * tt + (lane & 15)) * TP + (16 * i + 4 * g) * 2) = v;
        }
    }
    LGKM0();
    const int row = lane & 31, hh = lane >> 5, blk = 4 * r + w;
#pragma unroll 1
    for (int uu = 0; uu < 6; ++uu) {
        const int u = 6 * c + uu, type = u >> 5, h = u & 31, n0 = shift + 18 * uu;
        if (type < 2) {
            const lptr src = T + row * TP + (n0 + 8 * hh) * 2;
            u32x4 v;
#pragma unroll
            for (int e = 0; e < 4; ++e) v[e] = *(LAS unsigned*)(src + 4 * e);
            const unsigned ex = *(LAS unsigned*)(T + row * TP + (n0 + 16) * 2);
            *(u32x4*)(ws + (type ? WS_KF1 : WS_QF1) + (size_t)(h * 64 + blk) * 1024 + lane * 16) = v;
            if (type == 0) {
                if (hh == 0) ((unsigned*)(ws + WS_Q2))[h * SEQ + blk * 32 + row] = ex;
            } else {
                if (hh == 0) *(u32x4*)(ws + WS_K2X + (size_t)(h * SEQ + blk * 32 + row) * 16) = u32x4{ex, 0x3f803f80u, 0x00003f80u, 0u};
                float s = 0.f;
#pragma unroll
                for (int e = 0; e < 4; ++e) s += bflo(v[e]) * bflo(v[e]) + bfhi(v[e]) * bfhi(v[e]);
                if (hh == 0) s += bflo(ex) * bflo(ex) + bfhi(ex) * bfhi(ex);
                s = red32(s);
#pragma unroll
                for (int o = 16; o >= 1; o >>= 1) s = fmaxf(s, __shfl_xor(s, o));
                if (lane == 0) ((float*)(ws + WS_KMAX))[h * 64 + blk] = s;
            }
        } else {
            if (row < 18) {
#pragma unroll
                for (int s = 0; s < 2; ++s) {
                    u32x4 v;
#pragma unroll
                    for (int e = 0; e < 4; ++e) {
                        const int k0 = 16 * s + 8 * (e >> 1) + 4 * hh + 2 * (e & 1);
                        const unsigned lo = *(LAS u16*)(T + k0 * TP + (n0 + row) * 2), hi = *(LAS u16*)(T + (k0 + 1) * TP + (n0 + row) * 2);
                        v[e] = lo | (hi << 16);
                    }
                    *(u32x4*)(ws + WS_VT + (size_t)(((h * 64 + blk) * 2 + s) * 2 + hh) * 288 + row * 16) = v;
                }
            }
        }
    }
}

__global__ __launch_bounds__(256, 2) void k_l1(Params P, int base) {
    extern __shared__ __attribute__((aligned(16))) char smem[];
    const lptr lds = (lptr)smem;
    const unsigned lds0 = (unsigned)(uintptr_t)lds;
    const int w = __builtin_amdgcn_readfirstlane(threadIdx.x >> 6), lane = threadIdx.x & 63;
    const int bid = blockIdx.x + base;
    TL(0, 0);
    if (bid < 256) l1_role_g(P, lds, lds0, bid, w, lane);
    else l1_role_a(P, lds, lds0, bid - 256, w, lane);
    TL(0, 6);
}

DI void l2_role_g(const Params& P, lptr lds, unsigned lds0, int idx, int w, int lane) {
    const int x = idx & 7, y = idx >> 3;
    const int r = (x >> 1) * 4 + (y >> 3), j = (x & 1) * 8 + (y & 7);
    const int g = lane >> 4;
    char* ws = P.ws;
    Gemm<9, false> G; G.init(lds, lds0, w, lane);
    a_offsets_linear<9>(G.aoff, w, lane);
    b_offsets_rowmajor<4>(G.boff, 32 * w, lane, SEQ * 2);
    const Src1 S{ws + WS_Z2N + (size_t)j * 64 * 9216, 9216, (const char*)P.wsp + (size_t)r * 128 * SEQ * 2};
    G.prologue(S.a(0), S.a(1), S.a(2), S.b(0), S.b(1));
    TL(1, 1); TLC(1, 6);
    f32x4 acc[9][2];
    zero_acc<9>(acc);
    gemm_run(G, acc, S, 0, 32, 32);
    TL(1, 2); TLC(1, 7);
    const u32x4* z1p = (const u32x4*)(ws + WS_Z1) + (size_t)idx * 9 * 256 + w * 64 + lane;
    float bs[2];
#pragma unroll
    for (int tt = 0; tt < 2; ++tt) bs[tt] = __uint_as_float((unsigned)P.bsp[r * 128 + 32 * w + 16 * tt + (lane & 15)] << 16);
    WAIT_BAR0();
    const lptr st = lds + w * 9216;
#pragma unroll
    for (int i = 0; i < 9; ++i) {
        const u32x4 z = z1p[i * 256];
#pragma unroll
        for (int tt = 0; tt < 2; ++tt) {
            u32x2 v;
            v[0] = pk2(bflo(z[2 * tt]) * (acc[i][tt][0] + bs[tt]), bfhi(z[2 * tt]) * (acc[i][tt][1] + bs[tt]));
            v[1] = pk2(bflo(z[2 * tt + 1]) * (acc[i][tt][2] + bs[tt]), bfhi(z[2 * tt + 1]) * (acc[i][tt][3] + bs[tt]));
            *(LAS u32x2*)(st + (16 * tt + (lane & 15)) * 288 + (16 * i + 4 * g) * 2) = v;
        }
    }
    LGKM0();
    const int tb = 2 * r + (w >> 1);
#pragma unroll
    for (int n = 0; n < 9; ++n) {
        const int id = n * 64 + lane, row = id / 18, c = id - row * 18;
        const int cg = 18 * j + c, t64 = 32 * (w & 1) + row;
        *(u32x4*)(ws + WS_U + (size_t)(tb * 36 + (cg >> 3)) * 8192 + t64 * 128 + (((cg & 7) ^ (t64 & 7)) * 16)) = *(LAS u32x4*)(st + row * 288 + c * 16);
    }
}

constexpr int AT_CH = 14336, AT_K2 = 4096, AT_V = 6144;
DI void attn_issue(const char* ws, int h, int ci, unsigned ldsb, int w, int lane) {
    const char* k1 = ws + WS_KF1 + (size_t)(h * 64 + ci * 4) * 1024;
    const char* k2 = ws + WS_K2X + (size_t)(h * SEQ + ci * 128) * 16;
    const char* vt = ws + WS_VT + (size_t)(h * 64 + ci * 4) * 1152;
    glds(k1, (unsigned)(w * 1024 + lane * 16), ldsb + w * 1024);
    glds_m(k2, (unsigned)(w * 512 + (lane & 31) * 16), ldsb + AT_K2 + w * 512, 0xffffffffull);
    const unsigned vo = (unsigned)((lane >> 5) * 288 + (lane & 31) * 16);
#pragma unroll
    for (int n = 0; n < 2; ++n) glds_m(vt, vo + (unsigned)(w * 2 + n) * 576u, ldsb + AT_V + (w * 2 + n) * 1024, 0x0003ffff0003ffffull);
}
#define ZERO16 (f32x16{0, 0, 0, 0, 0, 0, 0, 0, 0, 0, 0, 0, 0, 0, 0, 0})
#define EXP2(x) __builtin_amdgcn_exp2f(x)
#define BF8(x) __builtin_bit_cast(bf16x8, x)
#define GRP(S, o_, Pv, j_) do { SB(); float e0_ = EXP2(S[o_]), e1_ = EXP2(S[o_ + 1]), e2_ = EXP2(S[o_ + 2]), e3_ = EXP2(S[o_ + 3]); \
    asm volatile("" : "+v"(e0_), "+v"(e1_), "+v"(e2_), "+v"(e3_)); Pv[j_] = pk2(e0_, e1_); Pv[j_ + 1] = pk2(e2_, e3_); PIN(Pv); SB(); } while (0)
DI void attn_step(f32x16 (&o)[2], f32x16& sA, u32x4& p1, bf16x8& kf1, bf16x8& kf2, bf16x8& vf1, const bf16x8 (&qf1)[2], const u32x4 (&qf2)[2],
                  lptr pv  , lptr pk1n  , lptr pk2n  ) {
    f32x16 sB; u32x4 p0;
    sB = MFMA32(kf1, qf1[1], ZERO16);
    const bf16x8 vf0 = *(LAS bf16x8*)(pv);                   GRP(sA, 0, p0, 0);
    o[1] = MFMA32(vf1, BF8(p1), o[1]);
    const bf16x8 nk1 = *(LAS bf16x8*)(pk1n);                 GRP(sA, 4, p0, 2);
    sB = MFMA32(kf2, BF8(qf2[1]), sB);
    const bf16x8 nv1 = *(LAS bf16x8*)(pv + 1024);            GRP(sA, 8, p1, 0);
    o[0] = MFMA32(vf0, BF8(p0), o[0]);
    const bf16x8 nk2 = *(LAS bf16x8*)(pk2n);                 GRP(sA, 12, p1, 2);
    sA = MFMA32(nk1, qf1[0], ZERO16);                        GRP(sB, 0, p0, 0);
    o[0] = MFMA32(nv1, BF8(p1), o[0]);                       GRP(sB, 4, p0, 2);
    sA = MFMA32(nk2, BF8(qf2[0]), sA);                       GRP(sB, 8, p1, 0);
    o[1] = MFMA32(vf0, BF8(p0), o[1]);                       GRP(sB, 12, p1, 2);
    kf1 = nk1; kf2 = nk2; vf1 = nv1;
}
DI void attn_sweep(f32x16 (&o)[2], const bf16x8 (&qf1)[2], const u32x4 (&qf2)[2], const char* ws, int h, lptr lds, unsigned lds0, int w, int lane) {
    const lptr pk1 = lds + lane * 16, pk2 = lds + AT_K2 + (lane & 31) * 16, pv = lds + AT_V + lane * 16;
    wait_bar<8>();
    bf16x8 kf1 = *(LAS bf16x8*)(pk1), kf2 = *(LAS bf16x8*)(pk2), vf1 = kf1;
    f32x16 sA = MFMA32(kf1, qf1[0], ZERO16);
    sA = MFMA32(kf2, BF8(qf2[0]), sA);
    u32x4 p1 = {0u, 0u, 0u, 0u};
#pragma unroll
    for (int b = 0; b < 2; ++b) o[b] = ZERO16;
    unsigned cur = 0;
    for (int ci = 0; ci < 16; ++ci) {
        const unsigned nxt = (cur + AT_CH == 4 * AT_CH) ? 0u : cur + AT_CH;
        attn_step(o, sA, p1, kf1, kf2, vf1, qf1, qf2, pv + cur, pk1 + cur + 1024, pk2 + cur + 512);
        attn_step(o, sA, p1, kf1, kf2, vf1, qf1, qf2, pv + cur + 2048, pk1 + cur + 2048, pk2 + cur + 1024);
        attn_step(o, sA, p1, kf1, kf2, vf1, qf1, qf2, pv + cur + 4096, pk1 + cur + 3072, pk2 + cur + 1536);
        if (ci + 2 < 16) wait_bar<4>(); else wait_bar<0>();
        if (ci + 3 < 16) attn_issue(ws, h, ci + 3, lds0 + ((ci + 3) & 3) * AT_CH, w, lane);
        const unsigned kn = (ci + 1 < 16) ? nxt : cur;
        attn_step(o, sA, p1, kf1, kf2, vf1, qf1, qf2, pv + cur + 6144, pk1 + kn, pk2 + kn);
        cur = nxt;
    }
    o[1] = MFMA32(vf1, BF8(p1), o[1]);
}
DI void attn_rowmax(float (&mx)[2], const bf16x8 (&qf1)[2], const u32x4 (&qf2)[2], const char* ws, int h, lptr lds, unsigned lds0, int w, int lane) {
    mx[0] = -INFINITY; mx[1] = -INFINITY;
    for (int ci = 0; ci < 16; ++ci) {
        WAIT_BAR0();
        attn_issue(ws, h, ci, lds0, w, lane);
        WAIT_BAR0();
        for (int kb = 0; kb < 4; ++kb) {
            const bf16x8 kf1 = *(LAS bf16x8*)(lds + lane * 16 + kb * 1024), kf2 = *(LAS bf16x8*)(lds + AT_K2 + (lane & 31) * 16 + kb * 512);
#pragma unroll
            for (int b = 0; b < 2; ++b) {
                f32x16 s = MFMA32(kf1, qf1[b], ZERO16);
                s = MFMA32(kf2, BF8(qf2[b]), s);
#pragma unroll
                for (int e = 0; e < 16; ++e) mx[b] = fmaxf(mx[b], s[e]);
            }
        }
    }
    WAIT_BAR0();
}
DI void l2_role_a(const Params& P, lptr lds, unsigned lds0, int idx, int w, int lane) {
    const int x = idx & 7, y = idx >> 3;
    const int h = x * 4 + (y >> 3), qc = y & 7;
    char* ws = P.ws;
    const int row = lane & 31, hh = lane >> 5;
    attn_issue(ws, h, 0, lds0, w, lane);
    attn_issue(ws, h, 1, lds0 + AT_CH, w, lane);
    attn_issue(ws, h, 2, lds0 + 2 * AT_CH, w, lane);
    for (int n = w; n < 32; n += 4)
        if (row >= 18) { const unsigned v = row == 18 ? 0x3f803f80u : 0u; *(LAS u32x4*)(lds + (n >> 3) * AT_CH + AT_V + (n & 7) * 1024 + lane * 16) = u32x4{v, v, v, v}; }
    float kmax2 = ((const float*)(ws + WS_KMAX))[h * 64 + lane];
#pragma unroll
    for (int o = 32; o >= 1; o >>= 1) kmax2 = fmaxf(kmax2, __shfl_xor(kmax2, o));
    bf16x8 qf1[2]; u32x4 qf2[2];
#pragma unroll
    for (int b = 0; b < 2; ++b) {
        const int qb = qc * 8 + w * 2 + b;
        const u32x4 v = *(const u32x4*)(ws + WS_QF1 + (size_t)(h * 64 + qb) * 1024 + lane * 16);
        const unsigned ex = ((const unsigned*)(ws + WS_Q2))[h * SEQ + qb * 32 + row];
        float s = 0.f;
#pragma unroll
        for (int e = 0; e < 4; ++e) s += bflo(v[e]) * bflo(v[e]) + bfhi(v[e]) * bfhi(v[e]);
        if (hh == 0) s += bflo(ex) * bflo(ex) + bfhi(ex) * bfhi(ex);
        s = red32(s);
        const float M = sqrtf(s * kmax2) * 1.0078125f;
        const unsigned mb = ((__float_as_uint(M) + 0xffffu) >> 16) | 0x8000u;
        qf1[b] = __builtin_bit_cast(bf16x8, v);
        qf2[b] = hh == 0 ? u32x4{ex, mb, 0u, 0u} : u32x4{0u, 0u, 0u, 0u};
    }
    f32x16 o[2];
    TL(1, 1); TLC(1, 6);
    attn_sweep(o, qf1, qf2, ws, h, lds, lds0, w, lane);
    TL(1, 2); TLC(1, 7);
    {
        bool bad = false;
#pragma unroll
        for (int b = 0; b < 2; ++b) { const float l = __shfl(o[b][10], row); bad |= !(l >= 8.271806125530277e-25f); }
        LAS unsigned* vote = (LAS unsigned*)(lds + 4 * AT_CH);
        if (threadIdx.x == 0) *vote = 0u;
        WAIT_BAR0();
        if (__any(bad) && lane == 0) *vote = 1u;
        WAIT_BAR0();
        if (__builtin_amdgcn_readfirstlane(*vote) != 0u) {
#pragma unroll
            for (int b = 0; b < 2; ++b) qf2[b][1] = 0u;
            float mx[2];
            attn_rowmax(mx, qf1, qf2, ws, h, lds, lds0, w, lane);
#pragma unroll
            for (int b = 0; b < 2; ++b) {
                const float nm = -max32(mx[b]);
                const unsigned h1 = __float_as_uint(nm) & 0xffff0000u; const float r1 = nm - __uint_as_float(h1);
                const unsigned h2 = __float_as_uint(r1) & 0xffff0000u; const float r2 = r1 - __uint_as_float(h2);
                const unsigned h3 = __float_as_uint(r2) & 0xffff0000u;
                if (hh == 0) { qf2[b][1] = (h1 >> 16) | h2; qf2[b][2] = h3 >> 16; }
            }
            attn_issue(ws, h, 0, lds0, w, lane);
            attn_issue(ws, h, 1, lds0 + AT_CH, w, lane);
            attn_issue(ws, h, 2, lds0 + 2 * AT_CH, w, lane);
            attn_sweep(o, qf1, qf2, ws, h, lds, lds0, w, lane);
            WAIT_BAR0();
        }
    }
    const lptr st = lds + w * 2560;
#pragma unroll
    for (int b = 0; b < 2; ++b) {
        const float inv = 1.0f / __shfl(o[b][10], row);
        const lptr rp = st + (32 * b + row) * 36 + hh * 8;
        *(LAS unsigned*)(rp + 0) = pk2(o[b][0] * inv, o[b][1] * inv);
        *(LAS unsigned*)(rp + 4) = pk2(o[b][2] * inv, o[b][3] * inv);
        *(LAS unsigned*)(rp + 16) = pk2(o[b][4] * inv, o[b][5] * inv);
        *(LAS unsigned*)(rp + 20) = pk2(o[b][6] * inv, o[b][7] * inv);
        if (hh == 0) *(LAS unsigned*)(rp + 32) = pk2(o[b][8] * inv, o[b][9] * inv);
    }
    LGKM0();
    char* ob = ws + WS_O + (size_t)(qc * 256 + w * 64) * (DM * 2) + h * 36;
#pragma unroll
    for (int n = 0; n < 9; ++n) {
        const int id = n * 64 + lane, rr = id / 9, cc = id - rr * 9;
        *(unsigned*)(ob + (size_t)rr * (DM * 2) + cc * 4) = *(LAS unsigned*)(st + id * 4);
    }
}

__global__ __launch_bounds__(256, 2) void k_l2(Params P, int base) {
    extern __shared__ __attribute__((aligned(16))) char smem[];
    const lptr lds = (lptr)smem;
    const unsigned lds0 = (unsigned)(uintptr_t)lds;
    const int w = __builtin_amdgcn_readfirstlane(threadIdx.x >> 6), lane = threadIdx.x & 63;
    const int bid = blockIdx.x + base;
    TL(1, 0);
    if (bid < 256) l2_role_g(P, lds, lds0, bid, w, lane);
    else l2_role_a(P, lds, lds0, bid - 256, w, lane);
    TL(1, 3);
}

constexpr int F_AB = 64 * 160, F_STAGE = F_AB + 8192;
struct FSrc { const char* a; const char* b; };
template <bool ISB>
DI void fstep(f32x4 (&acc)[5][2], lptr pa, lptr pb, bool doI, const char* sa, const char* sb, unsigned dst, const unsigned (&aoff)[3], unsigned bo0, unsigned bo1, int w) {
    const bf16x8 b0 = *(LAS bf16x8*)(pb), b1 = *(LAS bf16x8*)(pb + 2048);
    s16x4 lo[5], hi[5];
#pragma unroll
    for (int i = 0; i < 5; ++i) { lo[i] = trread(pa + 32 * i); hi[i] = trread(pa + 16 * 160 + 32 * i); }
    SB();
#pragma unroll
    for (int i = 0; i < 5; ++i) {
        const bf16x8 af = __builtin_shufflevector(lo[i], hi[i], 0, 1, 2, 3, 4, 5, 6, 7);
        acc[i][0] = MFMA16(af, b0, acc[i][0]);
        acc[i][1] = MFMA16(af, b1, acc[i][1]);
        if (doI) {
            if (i < 2) glds(sa, aoff[i], dst + (unsigned)(i * 4 + w) * 1024u);
            else if (i == 2) glds_m(sa, aoff[2], dst + 8192u + (unsigned)w * 512u, 0xffffffffull);
            else glds(sb, i == 3 ? bo0 : bo1, dst + F_AB + w * 2048 + (i - 3) * 1024);
        }
        SB();
    }
}
__global__ __launch_bounds__(256, 2) void k_l3(Params P) {
    extern __shared__ __attribute__((aligned(16))) char smem[];
    const lptr lds = (lptr)smem;
    const unsigned lds0 = (unsigned)(uintptr_t)lds;
    const int w = __builtin_amdgcn_readfirstlane(threadIdx.x >> 6), lane = threadIdx.x & 63;
    TL(2, 0);
    const int idx = blockIdx.x, x = idx & 7, y = idx >> 3;
    const int tb = x * 4 + (y >> 3), e = y & 7;
    const int d0 = e < 7 ? 72 * e : 496, skip = e < 7 ? 0 : 8;
    const int th = w & 1, kh = w >> 1, g = lane >> 4, q = (lane & 15) >> 2, p = lane & 3;
    char* ws = P.ws;
    unsigned aoff[3], boffo[2];
#pragma unroll
    for (int n = 0; n < 3; ++n) {
        const int id = n < 2 ? (n * 4 + w) * 64 + lane : 512 + w * 32 + (lane & 31), rho = id / 10, c = id - rho * 10;
        aoff[n] = (unsigned)rho2k(rho) * (DM * 2) + (unsigned)c * 16u;
    }
    const unsigned bu0 = (unsigned)(w * 2048 + lane * 16), bu1 = bu0 + 1024;
    b_offsets_rowmajor<2>(boffo, 16 * w, lane, DM * 2);
    const lptr pa = lds + (32 * kh + 4 * g + q) * 160 + 8 * p;
    const lptr pb = lds + F_AB + (32 * th + (lane & 15)) * 128 + (((4 * kh + g) ^ (lane & 7)) * 16);
    f32x4 accb[5][2], acca[5][2];
#pragma unroll
    for (int i = 0; i < 5; ++i) { accb[i][0] = f32x4{0, 0, 0, 0}; accb[i][1] = f32x4{0, 0, 0, 0}; acca[i][0] = f32x4{0, 0, 0, 0}; acca[i][1] = f32x4{0, 0, 0, 0}; }
    const char* wout = (const char*)P.wout + d0 * 2;
    const char* wo = (const char*)P.wo + d0 * 2;
    const char* ub = ws + WS_U + (size_t)tb * 36 * 8192;
    const char* obp = ws + WS_O + (size_t)tb * 64 * DM * 2;
#pragma unroll
    for (int k = 0; k < 3; ++k) {
        const unsigned dst = lds0 + k * F_STAGE;
        glds(wout + (size_t)k * 64 * DM * 2, aoff[0], dst + (unsigned)w * 1024u);
        glds(wout + (size_t)k * 64 * DM * 2, aoff[1], dst + (unsigned)(4 + w) * 1024u);
        glds_m(wout + (size_t)k * 64 * DM * 2, aoff[2], dst + 8192u + (unsigned)w * 512u, 0xffffffffull);
        glds(ub + k * 8192, bu0, dst + F_AB + w * 2048);
        glds(ub + k * 8192, bu1, dst + F_AB + w * 2048 + 1024);
    }
    TL(2, 1);
    for (int k = 0; k < 36; ++k) {
        wait_bar<10>();
        const int kn = k + 3;
        const bool nb = kn < 36;
        const char* sa = nb ? wout + (size_t)kn * 64 * DM * 2 : wo + (size_t)(kn - 36) * 64 * DM * 2;
        const char* sb = nb ? ub + (size_t)kn * 8192 : obp + (kn - 36) * 128;
        fstep<true>(accb, pa + (k & 3) * F_STAGE, pb + (k & 3) * F_STAGE, true, sa, sb, lds0 + (kn & 3) * F_STAGE, aoff, nb ? bu0 : boffo[0], nb ? bu1 : boffo[1], w);
    }
    for (int k = 36; k < 45; ++k) {
        if (k + 2 < 45) wait_bar<10>(); else wait_bar<0>();
        const int kn = k + 3;
        fstep<false>(acca, pa + (k & 3) * F_STAGE, pb + (k & 3) * F_STAGE, kn < 45, wo + (size_t)(kn - 36) * 64 * DM * 2, obp + (kn - 36) * 128, lds0 + (kn & 3) * F_STAGE, aoff, boffo[0], boffo[1], w);
    }
    TL(2, 2);
    float gt[2];
#pragma unroll
    for (int tt = 0; tt < 2; ++tt) gt[tt] = ((const float*)(ws + WS_GATE))[tb * 64 + 32 * th + 16 * tt + (lane & 15)];
#pragma unroll
    for (int i = 0; i < 5; ++i)
#pragma unroll
        for (int tt = 0; tt < 2; ++tt) accb[i][tt] = acca[i][tt] * gt[tt] + accb[i][tt] * (1.0f - gt[tt]);
    WAIT_BAR0();
    const lptr xch = lds + th * 10240 + lane * 16;
    if (kh == 1) {
#pragma unroll
        for (int i = 0; i < 5; ++i)
#pragma unroll
            for (int tt = 0; tt < 2; ++tt) *(LAS f32x4*)(xch + (i * 2 + tt) * 1024) = accb[i][tt];
    }
    WAIT_BAR0();
    const lptr st = lds + 20480;
    if (kh == 0) {
#pragma unroll
        for (int i = 0; i < 5; ++i)
#pragma unroll
            for (int tt = 0; tt < 2; ++tt) {
                const f32x4 v = accb[i][tt] + *(LAS f32x4*)(xch + (i * 2 + tt) * 1024);
                u32x2 o; o[0] = pk2(v[0], v[1]); o[1] = pk2(v[2], v[3]);
                *(LAS u32x2*)(st + (32 * th + 16 * tt + (lane & 15)) * 160 + (16 * i + 4 * g) * 2) = o;
            }
    }
    WAIT_BAR0();
    for (int id = threadIdx.x; id < 64 * 9; id += 256) {
        const int row = id / 9, c = id - row * 9;
        *(u32x4*)((char*)P.out + (size_t)(tb * 64 + row) * (DM * 2) + (d0 + skip) * 2 + c * 16) = *(LAS u32x4*)(st + row * 160 + skip * 2 + c * 16);
    }
    TL(2, 3);
}

extern "C" void kernel_launch(void* const* d_in, const int* in_sizes, int n_in, void* d_out, int out_size, void* d_ws, size_t ws_size, hipStream_t stream) {
    Params P;
    P.ax = (const u16*)d_in[0]; P.wqkv = (const u16*)d_in[1]; P.wo = (const u16*)d_in[2]; P.bx = (const u16*)d_in[3]; P.win = (const u16*)d_in[4];
    P.wsp = (const u16*)d_in[5]; P.bsp = (const u16*)d_in[6]; P.wout = (const u16*)d_in[7]; P.gw = (const u16*)d_in[8];
    P.out = (u16*)d_out; P.ws = (char*)d_ws;
    (void)hipFuncSetAttribute((const void*)k_l1, hipFuncAttributeMaxDynamicSharedMemorySize, LDS_BYTES);
    (void)hipFuncSetAttribute((const void*)k_l2, hipFuncAttributeMaxDynamicSharedMemorySize, LDS_BYTES);
    (void)hipFuncSetAttribute((const void*)k_l3, hipFuncAttributeMaxDynamicSharedMemorySize, LDS_BYTES);
    k_l1<<<512, 256, LDS_BYTES, stream>>>(P, 0);
    k_l2<<<512, 256, LDS_BYTES, stream>>>(P, 0);
    k_l3<<<256, 256, LDS_BYTES, stream>>>(P);
}
```

```cpp
#include <hip/hip_runtime.h>
#include <stdint.h>

typedef unsigned short u16;
typedef unsigned long long u64;
typedef short bf16x8 __attribute__((ext_vector_type(8)));
typedef short s16x4 __attribute__((ext_vector_type(4)));
typedef float f32x2 __attribute__((ext_vector_type(2)));
typedef float f32x4 __attribute__((ext_vector_type(4)));
typedef float f32x16 __attribute__((ext_vector_type(16)));
typedef unsigned u32x2 __attribute__((ext_vector_type(2)));
typedef unsigned u32x4 __attribute__((ext_vector_type(4)));
typedef __bf16 bf16x2_t __attribute__((ext_vector_type(2)));
#define LAS __attribute__((address_space(3)))
#define GAS __attribute__((address_space(1)))
typedef LAS char* lptr;
typedef GAS u64* gu64;
#define DI __device__ __forceinline__

constexpr int SEQ = 2048, DM = 576, FH = 2304;
#define EXP_REP 1

constexpr unsigned WS_Z2N = 0;
constexpr unsigned WS_U = WS_Z2N + 9437184;
constexpr unsigned WS_QF1 = WS_U + 9437184;
constexpr unsigned WS_Q2 = WS_QF1 + 2097152;
constexpr unsigned AT_CH = 21504, AT_K2 = 8192, AT_V = 12288;
constexpr unsigned WS_KV = WS_Q2 + 262144;
constexpr unsigned WS_KMAX = WS_KV + 32 * 8 * AT_CH;
constexpr unsigned WS_O = WS_KMAX + 8192;
constexpr unsigned WS_GATE = WS_O + 2359296;
constexpr unsigned WS_XBUF = WS_GATE + 8192;
constexpr unsigned WS_F1 = WS_XBUF + 262144;
constexpr unsigned WS_F2 = WS_F1 + 8192;
constexpr unsigned WS_F3 = WS_F2 + 8192;
constexpr unsigned WS_F4 = WS_F3 + 8192;
constexpr unsigned WS_F5 = WS_F4 + 8192;
constexpr unsigned WS_NONCE = WS_F5 + 8192;
constexpr unsigned WS_Z2R = WS_NONCE + 256;
constexpr unsigned WS_END = WS_Z2R + 9437184;

struct Params {
    const u16* ax; const u16* wqkv; const u16* wo; const u16* win; const u16* wsp;
    const u16* bsp; const u16* wout; const u16* gw; u16* out; char* ws;
};

DI unsigned pk2(float lo, float hi) { f32x2 v = {lo, hi}; return __builtin_bit_cast(unsigned, __builtin_convertvector(v, bf16x2_t)); }
DI float bflo(unsigned u) { return __uint_as_float(u << 16); }
DI float bfhi(unsigned u) { return __uint_as_float(u & 0xffff0000u); }
DI float red16(float x) { auto r = __builtin_amdgcn_permlane16_swap(__float_as_uint(x), __float_as_uint(x), false, false); return __uint_as_float(r[0]) + __uint_as_float(r[1]); }
DI float red32(float x) { auto r = __builtin_amdgcn_permlane32_swap(__float_as_uint(x), __float_as_uint(x), false, false); return __uint_as_float(r[0]) + __uint_as_float(r[1]); }
DI float max32(float x) { auto r = __builtin_amdgcn_permlane32_swap(__float_as_uint(x), __float_as_uint(x), false, false); return fmaxf(__uint_as_float(r[0]), __uint_as_float(r[1])); }
DI float gelu_tanh(float x) {
    constexpr float C0 = -2.0f * 0.7978845608028654f * 1.4426950408889634f, C1 = C0 * 0.044715f;
    const float t = x * x, p = __builtin_fmaf(t, C1, C0), e = __builtin_amdgcn_exp2f(x * p);
    return x * __builtin_amdgcn_rcpf(1.0f + e);
}
#define SB() __builtin_amdgcn_sched_barrier(0)
DI int opq(int x) { asm volatile("" : "+v"(x)); return x; }
#define WAIT_BAR0() asm volatile("s_waitcnt vmcnt(0) lgkmcnt(0)\n\ts_barrier" ::: "memory")
#define LGKM0() asm volatile("s_waitcnt lgkmcnt(0)" ::: "memory")
#define VM0() asm volatile("s_waitcnt vmcnt(0)" ::: "memory")
template <int N> DI void wait_bar() {
    asm volatile("" ::: "memory");
    __builtin_amdgcn_s_waitcnt((N & 15) | ((N >> 4) << 14) | (7 << 4));
    __builtin_amdgcn_s_barrier();
    asm volatile("" ::: "memory");
}
DI s16x4 trread(lptr p) { return __builtin_amdgcn_ds_read_tr16_b64_v4i16((LAS s16x4*)p); }
#define MFMA16(a, b, c) __builtin_amdgcn_mfma_f32_16x16x32_bf16((a), (b), (c), 0, 0, 0)
#define MFMA32(a, b, c) __builtin_amdgcn_mfma_f32_32x32x16_bf16((a), (b), (c), 0, 0, 0)
DI void m0_set(unsigned base, int imm) { asm volatile("s_add_u32 m0, %0, %1" :: "s"(base), "i"(imm) : "scc", "memory"); }
template <bool SC1> DI void dma(const void* sbase, unsigned voff) {
    if constexpr (SC1) asm volatile("global_load_lds_dwordx4 %0, %1 sc1" :: "v"(voff), "s"(sbase) : "memory");
    else asm volatile("global_load_lds_dwordx4 %0, %1" :: "v"(voff), "s"(sbase) : "memory");
}
template <bool SC1> DI void dma_now(const void* sbase, unsigned voff, unsigned lds_addr) { m0_set(lds_addr, 0); asm volatile("s_nop 0"); dma<SC1>(sbase, voff); }

DI __amdgpu_buffer_rsrc_t ws_rsrc(char* ws) { return __builtin_amdgcn_make_buffer_rsrc(ws, 0, WS_END, 0x00020000); }
DI void st16(__amdgpu_buffer_rsrc_t rs, unsigned off, u32x4 v) { __builtin_amdgcn_raw_buffer_store_b128(v, rs, off, 0, 16); }
DI void st4(__amdgpu_buffer_rsrc_t rs, unsigned off, unsigned v) { __builtin_amdgcn_raw_buffer_store_b32(v, rs, off, 0, 16); }
DI u32x4 ld16(__amdgpu_buffer_rsrc_t rs, unsigned off) { return __builtin_amdgcn_raw_buffer_load_b128(rs, off, 0, 16); }
DI unsigned ld4(__amdgpu_buffer_rsrc_t rs, unsigned off) { return __builtin_amdgcn_raw_buffer_load_b32(rs, off, 0, 16); }
DI u64 ldf(const char* ws, unsigned off) { return __hip_atomic_load((gu64)(ws + off), __ATOMIC_RELAXED, __HIP_MEMORY_SCOPE_AGENT); }
DI void stf(char* ws, unsigned off, u64 v) { __hip_atomic_store((gu64)(ws + off), v, __ATOMIC_RELAXED, __HIP_MEMORY_SCOPE_AGENT); }
#define DUMPADDR 81664
constexpr int LDS_DUMP = DUMPADDR;
DI void touch(unsigned lds0, const char* p) { m0_set(lds0, LDS_DUMP); asm volatile("s_nop 0\n\tglobal_load_lds_dword %0, off" :: "v"(p) : "memory"); }
DI void touch_sc1(unsigned lds0, const char* p) { m0_set(lds0, LDS_DUMP); asm volatile("s_nop 0\n\tglobal_load_lds_dword %0, off sc1" :: "v"(p) : "memory"); }
DI void publish(char* ws, unsigned flag_off, u64 tag, int lane) { VM0(); if (lane == 0) stf(ws, flag_off, tag); }
template <int SLEEP = 8> DI void await(const char* ws, unsigned flag_off, u64 tag) {
    for (unsigned spins = 0; spins < (1u << 16); ++spins) {
        if (__all(ldf(ws, flag_off) == tag)) break;
        __builtin_amdgcn_s_sleep(SLEEP);
    }
    __builtin_amdgcn_fence(__ATOMIC_ACQUIRE, "wavefront");
}

DI void acquire() { __builtin_amdgcn_fence(__ATOMIC_ACQUIRE, "agent"); VM0(); }
#define TL(n) do {} while (0)
#define TLC(n) do {} while (0)

DI int rho2k(int rho) { const int r = rho & 31; return (rho & 32) + 8 * ((r >> 2) & 3) + 4 * (r >> 4) + (r & 3); }

constexpr int LDS_BYTES = 81920;
constexpr int A_SLOT = 9216, B_RING = 4 * A_SLOT, B_SLOT = 16384;
template <int NI> DI void b_offsets_rowmajor(unsigned (&off)[NI], int rw, int lane, unsigned ldb) {
#pragma unroll
    for (int n = 0; n < NI; ++n) { const int row = rw + 8 * n + (lane >> 3); off[n] = (unsigned)row * ldb + (unsigned)(((lane & 7) ^ (row & 7)) * 16); }
}
template <int NF, bool ATR> struct Gemm {
    static constexpr int P = NF * 32, NCH = NF * 2;
    static constexpr int NBASE = (NF * 2 * 32 / 64) / 4;
    static constexpr int NS = NBASE + 2;
    unsigned aoff[NBASE], aext, boff[4];
    lptr pa, pb0, pb1;
    unsigned dA, dAx, dB;
    int w;
    bf16x8 af[NF], bc[2];
    DI void init(lptr lds, unsigned lds0, int w_, int lane) {
        const int g = lane >> 4, q = (lane & 15) >> 2, p = lane & 3;
        w = w_;
        pa = ATR ? lds + (4 * g + q) * P + 8 * p : lds + lane * 16;
        pb0 = lds + B_RING + (32 * w + (lane & 15)) * 128 + ((g ^ (lane & 7)) * 16);
        pb1 = lds + B_RING + (32 * w + (lane & 15)) * 128 + (((4 + g) ^ (lane & 7)) * 16);
        dA = lds0 + w * 1024; dB = lds0 + B_RING + w * 4096;
        dAx = lds0 + NBASE * 4096 + (NF == 9 ? 0 : w * 1024);
    }
    DI void set_a(int lane, unsigned ldb) {
#pragma unroll
        for (int n = 0; n <= NBASE; ++n) {
            const int id = n < NBASE ? (n * 4 + w) * 64 + lane : NBASE * 256 + (NF == 9 ? 0 : (w < 3 ? w : 0) * 64) + lane;
            unsigned o;
            if constexpr (ATR) { const int rho = id / NCH, c = id - rho * NCH; o = (unsigned)rho2k(rho) * ldb + (unsigned)c * 16u; } else o = (unsigned)id * 16u;
            if (n < NBASE) aoff[n] = o; else aext = o;
        }
    }
    DI bool has_extra(int slot_idx) const { return NF == 9 ? w == slot_idx : w < 3; }
    DI bf16x8 read_a(lptr p, int off, int i) const {
        if constexpr (ATR) { const s16x4 lo = trread(p + off + 32 * i), hi = trread(p + off + 16 * P + 32 * i); return __builtin_shufflevector(lo, hi, 0, 1, 2, 3, 4, 5, 6, 7); }
        else return *(LAS bf16x8*)(p + off + 1024 * i);
    }
    DI void issue_a(const char* src, int si) const {
#pragma unroll
        for (int n = 0; n < NBASE; ++n) { m0_set(dA, si * A_SLOT + n * 4096); asm volatile("s_nop 0"); dma<false>(src, aoff[n]); }
        if (has_extra(si)) { m0_set(dAx, si * A_SLOT); asm volatile("s_nop 0"); dma<false>(src, aext); }
    }
    DI void issue_b(const char* src, int si, int n0) const {
#pragma unroll
        for (int n = n0; n < n0 + 2; ++n) { m0_set(dB, si * B_SLOT + n * 1024); asm volatile("s_nop 0"); dma<false>(src, boff[n]); }
    }
    DI void pro_start(const char* a0, const char* a1, const char* a2, const char* b0, const char* b1) const {
        issue_b(b0, 0, 0); issue_a(a0, 0); issue_b(b0, 0, 2); issue_a(a1, 1); issue_b(b1, 1, 0); issue_a(a2, 2);
    }
    DI void pro_finish() {
        wait_bar<2 * NBASE + 2>();
#pragma unroll
        for (int i = 0; i < NF - 2; ++i) af[i] = read_a(pa, 0, i);
        bc[0] = *(LAS bf16x8*)(pb0); bc[1] = *(LAS bf16x8*)(pb0 + 2048);
    }
    template <int SC, int SBI, int NWAIT, int NMID, bool DOB, bool DOA>
    DI void substep(f32x4 (&acc)[NF][2], const char* srcB, const char* srcA) {
        constexpr bool ODD = (SC & 1) != 0;
        constexpr int SN = (SC + 1) & 3, ST = (SC + 3) & 3;
        constexpr int B0 = ODD ? 0 : 2, BT = SBI ^ (ODD ? 0 : 1);
        constexpr int STEP = NF == 9 ? 2 : 1;
        wait_bar<NWAIT>();
        const lptr pbn = ODD ? pb0 + (SBI ^ 1) * B_SLOT : pb1 + SBI * B_SLOT;
        af[NF - 2] = read_a(pa, SC * A_SLOT, NF - 2);
        af[NF - 1] = read_a(pa, SC * A_SLOT, NF - 1);
        bf16x8 bn[2];
#pragma unroll
        for (int i = 0; i < NF; ++i) {
            const int d = (i % STEP == 0) ? i / STEP : -1;
            const bool isB = DOB && d >= 0 && d < 2, isA = DOA && d >= 2 && d < 2 + NBASE;
            if (isB) m0_set(dB, BT * B_SLOT + (B0 + d) * 1024);
            if (isA) m0_set(dA, ST * A_SLOT + (d - 2) * 4096);
            if (i == 1) bn[0] = *(LAS bf16x8*)(pbn);
            SB();
            acc[i][0] = MFMA16(af[i], bc[0], acc[i][0]);
            SB();
            if (isB) dma<false>(srcB, boff[B0 + d]);
            if (isA) dma<false>(srcA, aoff[d - 2]);
            SB();
            acc[i][1] = MFMA16(af[i], bc[1], acc[i][1]);
            SB();
            if (i < NF - 2) af[i] = read_a(pa, SN * A_SLOT, i);
            if (i == NF - 2) {
                if (NMID >= 0) asm volatile("s_waitcnt vmcnt(%0)" :: "n"(NMID < 0 ? 0 : NMID) : "memory");
                bn[1] = *(LAS bf16x8*)(pbn + 2048);
            }
            SB();
        }
        if (DOA) { if (has_extra(ST)) { m0_set(dAx, ST * A_SLOT); asm volatile("s_nop 0"); dma<false>(srcA, aext); } }
        bc[0] = bn[0]; bc[1] = bn[1];
    }
    template <int MODE, int PAR>
    DI void kstep(f32x4 (&acc)[NF][2], const char* bE, const char* aE, const char* bO, const char* aO) {
        substep<2 * PAR, PAR, MODE == 2 ? 0 : NS, -1, MODE != 2, MODE != 2>(acc, bE, aE);
        substep<2 * PAR + 1, PAR, MODE == 2 ? 0 : NS, MODE == 0 ? NBASE + NS : (MODE == 1 ? NBASE : -1), MODE == 0, MODE == 0>(acc, bO, aO);
    }
};
template <int PAR0, int NF, bool ATR>
DI void gemm_steady(Gemm<NF, ATR>& G, f32x4 (&acc)[NF][2], const char*& a, size_t asub, const char*& b, int k0, int k1) {
    int k = k0;
    if (PAR0 == 1 && k < k1) { G.template kstep<0, 1>(acc, b, a, b + 128, a + asub); a += 2 * asub; b += 128; ++k; }
    for (; k + 1 < k1; k += 2) {
        G.template kstep<0, 0>(acc, b, a, b + 128, a + asub); a += 2 * asub; b += 128;
        G.template kstep<0, 1>(acc, b, a, b + 128, a + asub); a += 2 * asub; b += 128;
    }
    if (k < k1) { G.template kstep<0, 0>(acc, b, a, b + 128, a + asub); a += 2 * asub; b += 128; }
}
template <int NF> DI void zero_acc(f32x4 (&acc)[NF][2]) {
#pragma unroll
    for (int i = 0; i < NF; ++i) { acc[i][0] = f32x4{0, 0, 0, 0}; acc[i][1] = f32x4{0, 0, 0, 0}; }
}

constexpr int F_AB = 64 * 144, F_STAGE = F_AB + 8192;
struct FFrag { bf16x8 a[5], b[2]; };
DI void fread(FFrag& f, lptr pa, lptr pb) {
    f.b[0] = *(LAS bf16x8*)(pb); f.b[1] = *(LAS bf16x8*)(pb + 2048);
#pragma unroll
    for (int i = 0; i < 5; ++i) { const s16x4 lo = trread(pa + 32 * i), hi = trread(pa + 16 * 144 + 32 * i); f.a[i] = __builtin_shufflevector(lo, hi, 0, 1, 2, 3, 4, 5, 6, 7); }
}
template <int SL, int NWAIT, bool DOI>
DI void fstep(f32x4 (&acc)[5][2], FFrag& f, lptr pa, lptr pb, const char* sa, const char* sb, unsigned dA, unsigned dB, unsigned dX, const unsigned (&aoff)[3], unsigned bo0, unsigned bo1, int w) {
    wait_bar<NWAIT>();
    constexpr int SN = (SL + 1) & 3;
    FFrag n;
#pragma unroll
    for (int i = 0; i < 5; ++i) {
        if (DOI && i < 2) m0_set(dA, SL * F_STAGE + i * 4096);
        if (DOI && i >= 2 && i < 4) m0_set(dB, SL * F_STAGE + (i - 2) * 1024);
        if (i < 2) n.b[i] = *(LAS bf16x8*)(pb + SN * F_STAGE + i * 2048);
        SB();
        acc[i][0] = MFMA16(f.a[i], f.b[0], acc[i][0]);
        SB();
        if (DOI && i < 2) dma<false>(sa, aoff[i]);
        if (DOI && i >= 2 && i < 4) dma<false>(sb, i == 2 ? bo0 : bo1);
        { const s16x4 lo = trread(pa + SN * F_STAGE + 32 * i), hi = trread(pa + SN * F_STAGE + 16 * 144 + 32 * i); n.a[i] = __builtin_shufflevector(lo, hi, 0, 1, 2, 3, 4, 5, 6, 7); }
        SB();
        acc[i][1] = MFMA16(f.a[i], f.b[1], acc[i][1]);
        SB();
    }
    if (DOI && w == SL) { m0_set(dX, SL * F_STAGE); asm volatile("s_nop 0"); dma<false>(sa, aoff[2]); }
    f = n;
}
struct FinalCtx { unsigned aoff[3]; };
DI void final_aoff(unsigned (&aoff)[3], int w, int lane) {
#pragma unroll
    for (int n = 0; n < 3; ++n) {
        const int id = n < 2 ? (n * 4 + w) * 64 + lane : 512 + lane, rho = id / 9, c = id - rho * 9;
        aoff[n] = (unsigned)rho2k(rho) * (DM * 2) + (unsigned)c * 16u;
    }
}
DI void final_weights(const Params& P, unsigned lds0, int e, int k, int w, const unsigned (&aoff)[3]) {
    const char* src = (const char*)P.wout + 72 * e * 2 + (size_t)k * 64 * DM * 2;
    dma_now<false>(src, aoff[0], lds0 + k * F_STAGE + w * 1024);
    dma_now<false>(src, aoff[1], lds0 + k * F_STAGE + (4 + w) * 1024);
    if (w == k) dma_now<false>(src, aoff[2], lds0 + k * F_STAGE + 8192);
}
DI void final_tile(const Params& P, __amdgpu_buffer_rsrc_t rs, u64 tag, lptr lds, unsigned lds0, int tb, int e, int w, int lane) {
    const int d0 = 72 * e;
    const int th = w & 1, kh = w >> 1;
    int g = lane >> 4; const int q = (lane & 15) >> 2, p = lane & 3;
    char* ws = P.ws;
    unsigned aoff[3], boffo[2];
    final_aoff(aoff, w, lane);
    const unsigned bu0 = (unsigned)(w * 2048 + lane * 16), bu1 = bu0 + 1024;
    b_offsets_rowmajor<2>(boffo, 16 * w, lane, DM * 2);
    const lptr pa = lds + (32 * kh + 4 * g + q) * 144 + 8 * p;
    const lptr pb = lds + F_AB + (32 * th + (lane & 15)) * 128 + (((4 * kh + g) ^ (lane & 7)) * 16);
    const unsigned dA = lds0 + w * 1024, dB = lds0 + F_AB + w * 2048, dX = lds0 + 8192;
    f32x4 accb[5][2], acca[5][2];
    zero_acc<5>(accb); zero_acc<5>(acca);
    const char* wout = (const char*)P.wout + d0 * 2;
    const char* wo = (const char*)P.wo + d0 * 2;
    const char* ub = ws + WS_U + (size_t)tb * 36 * 8192;
    const char* obp = ws + WS_O + (size_t)tb * 64 * DM * 2;
    final_weights(P, lds0, e, 2, w, aoff);
    final_weights(P, lds0, e, 3, w, aoff);
    if (w == 0) { await(ws, WS_F3 + (unsigned)(((tb >> 1) * 4 + 2 * (tb & 1)) * 16 + (lane & 31)) * 8, tag); acquire(); }
    WAIT_BAR0();
#pragma unroll
    for (int k = 0; k < 4; ++k) {
        dma_now<false>(ub + k * 8192, bu0, dB + k * F_STAGE);
        dma_now<false>(ub + k * 8192, bu1, dB + k * F_STAGE + 1024);
    }
    TL(9);
    FFrag f;
    wait_bar<6>();
    fread(f, pa, pb);
#define FSRC(kn) ((kn) < 36 ? wout + (size_t)(kn) * 64 * DM * 2 : wo + (size_t)((kn) - 36) * 64 * DM * 2), ((kn) < 36 ? ub + (size_t)(kn) * 8192 : obp + ((kn) - 36) * 128)
#define FB0(kn) ((kn) < 36 ? bu0 : boffo[0]), ((kn) < 36 ? bu1 : boffo[1])
    fstep<0, 4, true>(accb, f, pa, pb, FSRC(4), dA, dB, dX, aoff, FB0(4), w);
    fstep<1, 2 + 4, true>(accb, f, pa, pb, FSRC(5), dA, dB, dX, aoff, FB0(5), w);
    fstep<2, 8, true>(accb, f, pa, pb, FSRC(6), dA, dB, dX, aoff, FB0(6), w);
    fstep<3, 8, true>(accb, f, pa, pb, FSRC(7), dA, dB, dX, aoff, FB0(7), w);
    for (int k = 4; k < 32; k += 4) {
        fstep<0, 8, true>(accb, f, pa, pb, wout + (size_t)(k + 4) * 64 * DM * 2, ub + (size_t)(k + 4) * 8192, dA, dB, dX, aoff, bu0, bu1, w);
        fstep<1, 8, true>(accb, f, pa, pb, wout + (size_t)(k + 5) * 64 * DM * 2, ub + (size_t)(k + 5) * 8192, dA, dB, dX, aoff, bu0, bu1, w);
        fstep<2, 8, true>(accb, f, pa, pb, wout + (size_t)(k + 6) * 64 * DM * 2, ub + (size_t)(k + 6) * 8192, dA, dB, dX, aoff, bu0, bu1, w);
        fstep<3, 8, true>(accb, f, pa, pb, wout + (size_t)(k + 7) * 64 * DM * 2, ub + (size_t)(k + 7) * 8192, dA, dB, dX, aoff, bu0, bu1, w);
    }
    if (w == 0) { await(ws, WS_F4 + (unsigned)(tb * 32 + (lane & 31)) * 8, tag); acquire(); }
    asm volatile("s_barrier" ::: "memory");
    fstep<0, 8, true>(accb, f, pa, pb, FSRC(36), dA, dB, dX, aoff, FB0(36), w);
    fstep<1, 8, true>(accb, f, pa, pb, FSRC(37), dA, dB, dX, aoff, FB0(37), w);
    fstep<2, 8, true>(accb, f, pa, pb, FSRC(38), dA, dB, dX, aoff, FB0(38), w);
    fstep<3, 8, true>(accb, f, pa, pb, FSRC(39), dA, dB, dX, aoff, FB0(39), w);
    fstep<0, 8, true>(acca, f, pa, pb, FSRC(40), dA, dB, dX, aoff, FB0(40), w);
    fstep<1, 8, true>(acca, f, pa, pb, FSRC(41), dA, dB, dX, aoff, FB0(41), w);
    fstep<2, 8, true>(acca, f, pa, pb, FSRC(42), dA, dB, dX, aoff, FB0(42), w);
    fstep<3, 8, true>(acca, f, pa, pb, FSRC(43), dA, dB, dX, aoff, FB0(43), w);
    fstep<0, 8, true>(acca, f, pa, pb, FSRC(44), dA, dB, dX, aoff, FB0(44), w);
    fstep<1, 8, false>(acca, f, pa, pb, nullptr, nullptr, dA, dB, dX, aoff, 0, 0, w);
    fstep<2, 4, false>(acca, f, pa, pb, nullptr, nullptr, dA, dB, dX, aoff, 0, 0, w);
    fstep<3, 0, false>(acca, f, pa, pb, nullptr, nullptr, dA, dB, dX, aoff, 0, 0, w);
    fstep<0, 0, false>(acca, f, pa, pb, nullptr, nullptr, dA, dB, dX, aoff, 0, 0, w);
    TL(10);
    lane = opq(lane); g = lane >> 4;
    float gt[2];
#pragma unroll
    for (int tt = 0; tt < 2; ++tt) gt[tt] = __uint_as_float(ld4(rs, WS_GATE + (unsigned)(tb * 64 + 32 * th + 16 * tt + (lane & 15)) * 4));
#pragma unroll
    for (int i = 0; i < 5; ++i)
#pragma unroll
        for (int tt = 0; tt < 2; ++tt) accb[i][tt] = acca[i][tt] * gt[tt] + accb[i][tt] * (1.0f - gt[tt]);
    WAIT_BAR0();
    const lptr xch = lds + th * 10240 + lane * 16;
    if (kh == 1) {
#pragma unroll
        for (int i = 0; i < 5; ++i)
#pragma unroll
            for (int tt = 0; tt < 2; ++tt) *(LAS f32x4*)(xch + (i * 2 + tt) * 1024) = accb[i][tt];
    }
    WAIT_BAR0();
    const lptr st = lds + 20480;
    if (kh == 0) {
#pragma unroll
        for (int i = 0; i < 5; ++i)
#pragma unroll
            for (int tt = 0; tt < 2; ++tt) {
                const f32x4 v = accb[i][tt] + *(LAS f32x4*)(xch + (i * 2 + tt) * 1024);
                u32x2 o; o[0] = pk2(v[0], v[1]); o[1] = pk2(v[2], v[3]);
                *(LAS u32x2*)(st + (32 * th + 16 * tt + (lane & 15)) * 160 + (16 * i + 4 * g) * 2) = o;
            }
    }
    WAIT_BAR0();
    for (int id = threadIdx.x; id < 64 * 9; id += 256) {
        const int row = id / 9, c = id - row * 9;
        *(u32x4*)((char*)P.out + (size_t)(tb * 64 + row) * (DM * 2) + d0 * 2 + c * 16) = *(LAS u32x4*)(st + row * 160 + c * 16);
    }
}

DI void role_g(const Params& P, u64 tag, lptr lds, unsigned lds0, int idx, int w, int lane0) {
    const int x = idx & 7, y = idx >> 3;
    const int r = (x >> 1) * 4 + (y >> 3), j = (x & 1) * 8 + (y & 7);
    int lane = lane0;
    char* ws = P.ws;
    const __amdgpu_buffer_rsrc_t rs = ws_rsrc(ws);
    f32x4 acc[9][2];
    unsigned z1[9][2][2];
#define PARK ((u32x4*)(ws + WS_Z2R) + (size_t)idx * 9 * 256 + w * 64 + lane)
    {
        Gemm<9, true> G; G.init(lds, lds0, w, lane);
        G.set_a(lane, 4608 * 2);
        b_offsets_rowmajor<4>(G.boff, 32 * w, lane, DM * 2);
        constexpr size_t WSUB = (size_t)32 * 4608 * 2;
        const char* wz2 = (const char*)P.win + (size_t)(FH + 144 * j) * 2;
        const char* wz1 = (const char*)P.win + (size_t)(144 * j) * 2;
        const char* xb = (const char*)P.ax + (size_t)r * 128 * DM * 2;
        G.pro_start(wz2, wz2 + WSUB, wz2 + 2 * WSUB, xb, xb + 128);
        {
            const int row = r * 128 + j * 8 + (w * 2 + (lane >> 5)), l32 = lane & 31;
            float s = 0.f;
#pragma unroll
            for (int n = 0; n < 3; ++n) {
                const int c = l32 + 32 * n;
                if (c < 72) {
                    const u32x4 xv = *(const u32x4*)(P.ax + (size_t)row * DM + c * 8), gv = *(const u32x4*)(P.gw + c * 8);
#pragma unroll
                    for (int e = 0; e < 4; ++e) s += bflo(xv[e]) * bflo(gv[e]) + bfhi(xv[e]) * bfhi(gv[e]);
                }
            }
#pragma unroll
            for (int o = 16; o >= 1; o >>= 1) s += __shfl_xor(s, o);
            if (l32 == 0) st4(rs, WS_GATE + row * 4, __float_as_uint(__builtin_amdgcn_rcpf(1.0f + __builtin_amdgcn_exp2f(-1.4426950408889634f * s))));
        }
        G.pro_finish();
        TL(1);
        zero_acc<9>(acc);
        {
            const char* a = wz2 + 3 * WSUB; const char* b = xb + 128;
            gemm_steady<0>(G, acc, a, WSUB, b, 0, 7);
            G.template kstep<0, 1>(acc, xb + 8 * 128, wz2 + 17 * WSUB, xb, wz1);
            G.template kstep<0, 0>(acc, xb, wz1 + WSUB, xb + 128, wz1 + 2 * WSUB);
        }
        TL(2);
        lane = opq(lane0);
        {
            float mean[2], m2[2];
#pragma unroll
            for (int tt = 0; tt < 2; ++tt) {
                float s = 0.f;
#pragma unroll
                for (int i = 0; i < 9; ++i)
#pragma unroll
                    for (int e = 0; e < 4; ++e) { acc[i][tt][e] = gelu_tanh(acc[i][tt][e]); s += acc[i][tt][e]; }
                s = red32(red16(s));
                mean[tt] = s * (1.0f / 144.0f);
                float qq = 0.f;
#pragma unroll
                for (int i = 0; i < 9; ++i) {
#pragma unroll
                    for (int e = 0; e < 4; ++e) { const float d = acc[i][tt][e] - mean[tt]; qq = __builtin_fmaf(d, d, qq); }
                }
                m2[tt] = red32(red16(qq));
            }
#pragma unroll
            for (int i = 0; i < 9; ++i)
                PARK[i * 256] = u32x4{pk2(acc[i][0][0], acc[i][0][1]), pk2(acc[i][0][2], acc[i][0][3]), pk2(acc[i][1][0], acc[i][1][1]), pk2(acc[i][1][2], acc[i][1][3])};
            if (lane < 32) {
                const int tt = lane >> 4;
                const float mu = tt ? mean[1] : mean[0], mm = tt ? m2[1] : m2[0];
                stf(ws, WS_XBUF + (unsigned)((r * 16 + j) * 128 + 32 * w + lane) * 8, ((u64)__float_as_uint(mm) << 32) | __float_as_uint(mu));
            }
            publish(ws, WS_F1 + (unsigned)((r * 4 + w) * 16 + j) * 8, tag, lane);
        }
        TL(3);
        zero_acc<9>(acc);
        {
            const char* a = wz1 + 3 * WSUB; const char* b = xb + 128;
            gemm_steady<1>(G, acc, a, WSUB, b, 9, 16);
            G.template kstep<1, 0>(acc, xb + 8 * 128, wz1 + 17 * WSUB, nullptr, nullptr);
            G.template kstep<2, 1>(acc, nullptr, nullptr, nullptr, nullptr);
        }
        TL(4);
    }
#define Z1_GELU(i0, i1) _Pragma("unroll") for (int i = i0; i < i1; ++i) _Pragma("unroll") for (int tt = 0; tt < 2; ++tt) { \
        z1[i][tt][0] = pk2(gelu_tanh(acc[i][tt][0]), gelu_tanh(acc[i][tt][1])); z1[i][tt][1] = pk2(gelu_tanh(acc[i][tt][2]), gelu_tanh(acc[i][tt][3])); }
    lane = opq(lane0);
    const int g = lane >> 4;
    float mu[2], rstd2[2];
    u32x4 zp[9];
    {
        const unsigned f1 = WS_F1 + (unsigned)((r * 4 + w) * 16 + (lane & 15)) * 8;
        const u64 first = ldf(ws, f1);
        SB();
        Z1_GELU(0, 4);
        SB();
        if (!__all(first == tag)) await(ws, f1, tag);
        __builtin_amdgcn_fence(__ATOMIC_ACQUIRE, "wavefront");
        const unsigned xo = WS_XBUF + (unsigned)((r * 16 + (lane >> 5) * 8) * 128 + 32 * w + (lane & 31)) * 8;
        u64 sv[8];
#pragma unroll
        for (int e = 0; e < 8; ++e) sv[e] = ldf(ws, xo + e * 1024);
        SB();
        Z1_GELU(4, 9);
        SB();
#pragma unroll
        for (int i = 0; i < 9; ++i) zp[i] = PARK[i * 256];
        float m[8], q2[8], sm = 0.f;
#pragma unroll
        for (int e = 0; e < 8; ++e) { m[e] = __uint_as_float((unsigned)sv[e]); q2[e] = __uint_as_float((unsigned)(sv[e] >> 32)); sm += m[e]; }
        const float mean = red32(sm) * (1.0f / 16.0f);
        float qq = 0.f;
#pragma unroll
        for (int e = 0; e < 8; ++e) { const float d = m[e] - mean; qq += q2[e] + 144.0f * d * d; }
        qq = red32(qq);
        const float rstd = 1.0f / sqrtf(qq * (1.0f / 2304.0f) + 1e-5f);
#pragma unroll
        for (int tt = 0; tt < 2; ++tt) { mu[tt] = __shfl(mean, 16 * tt + (lane & 15)); rstd2[tt] = __shfl(rstd, 16 * tt + (lane & 15)); }
    }
    WAIT_BAR0();
    {
        const lptr st = lds + w * 9216;
#pragma unroll
        for (int tt = 0; tt < 2; ++tt) {
            const int gp = 2 * tt + ((lane & 15) >> 3), h = (lane >> 2) & 1, ii = lane & 3;
            const lptr rowp = st + (16 * h + 4 * gp + ii) * 288 + 8 * g;
            const float a = rstd2[tt], b = -mu[tt] * rstd2[tt];
#pragma unroll
            for (int i = 0; i < 9; ++i) {
                u32x2 v;
                v[0] = pk2(__builtin_fmaf(bflo(zp[i][2 * tt]), a, b), __builtin_fmaf(bfhi(zp[i][2 * tt]), a, b));
                v[1] = pk2(__builtin_fmaf(bflo(zp[i][2 * tt + 1]), a, b), __builtin_fmaf(bfhi(zp[i][2 * tt + 1]), a, b));
                *(LAS u32x2*)(rowp + 32 * i) = v;
            }
        }
        LGKM0();
        const lptr rd = st + (4 * g + ((lane & 15) >> 2)) * 288 + 8 * (lane & 3);
        const unsigned dst = WS_Z2N + (unsigned)(j * 64 + 4 * r + w) * 9216 + lane * 16;
#pragma unroll
        for (int i = 0; i < 9; ++i) {
            const s16x4 lo = trread(rd + 32 * i), hi = trread(rd + 16 * 288 + 32 * i);
            st16(rs, dst + i * 1024, __builtin_bit_cast(u32x4, __builtin_shufflevector(lo, hi, 0, 1, 2, 3, 4, 5, 6, 7)));
        }
    }
    publish(ws, WS_F2 + (unsigned)(j * 64 + 4 * r + w) * 8, tag, lane);
    TL(5);
    lane = opq(lane0);
    u16 bsv[2];
#pragma unroll
    for (int tt = 0; tt < 2; ++tt) bsv[tt] = P.bsp[r * 128 + 32 * w + 16 * tt + (lane & 15)];
    {
        Gemm<9, false> G; G.init(lds, lds0, w, lane);
        G.set_a(lane, 0);
        b_offsets_rowmajor<4>(G.boff, 32 * w, lane, SEQ * 2);
        const char* zb = ws + WS_Z2N + (size_t)j * 64 * 9216;
        const char* wb = (const char*)P.wsp + (size_t)r * 128 * SEQ * 2;
        if (w == 0) { await(ws, WS_F2 + (unsigned)(j * 64 + lane) * 8, tag); acquire(); }
        WAIT_BAR0();
        TL(6);
        for (int rep = 0; rep < EXP_REP; ++rep) {
            if (rep) WAIT_BAR0();
            G.pro_start(zb, zb + 9216, zb + 2 * 9216, wb, wb + 128);
            G.pro_finish();
            zero_acc<9>(acc);
            TLC(12);
            const char* a = zb + 3 * 9216; const char* b = wb + 128;
            gemm_steady<0>(G, acc, a, (size_t)9216, b, 0, 30);
            G.template kstep<1, 0>(acc, b, a, nullptr, nullptr);
            G.template kstep<2, 1>(acc, nullptr, nullptr, nullptr, nullptr);
        }
        TLC(13);
    }
    TL(7);
    lane = opq(lane0);
    {
        const int g = lane >> 4;
        float bs[2];
#pragma unroll
        for (int tt = 0; tt < 2; ++tt) bs[tt] = __uint_as_float((unsigned)bsv[tt] << 16);
        WAIT_BAR0();
        {
            unsigned fa[3];
            final_aoff(fa, w, lane);
            final_weights(P, lds0, j & 7, 0, w, fa);
            final_weights(P, lds0, j & 7, 1, w, fa);
        }
        const lptr st = lds + 45056 + w * 9216;
#pragma unroll
        for (int i = 0; i < 9; ++i)
#pragma unroll
            for (int tt = 0; tt < 2; ++tt) {
                u32x2 v;
                v[0] = pk2(bflo(z1[i][tt][0]) * (acc[i][tt][0] + bs[tt]), bfhi(z1[i][tt][0]) * (acc[i][tt][1] + bs[tt]));
                v[1] = pk2(bflo(z1[i][tt][1]) * (acc[i][tt][2] + bs[tt]), bfhi(z1[i][tt][1]) * (acc[i][tt][3] + bs[tt]));
                *(LAS u32x2*)(st + (16 * tt + (lane & 15)) * 288 + (16 * i + 4 * g) * 2) = v;
            }
        LGKM0();
        const int tb = 2 * r + (w >> 1);
#pragma unroll
        for (int n = 0; n < 9; ++n) {
            const int id = n * 64 + lane, row = id / 18, c = id - row * 18;
            const int cg = 18 * j + c, t64 = 32 * (w & 1) + row;
            st16(rs, WS_U + (unsigned)(tb * 36 + (cg >> 3)) * 8192 + t64 * 128 + (((cg & 7) ^ (t64 & 7)) * 16), *(LAS u32x4*)(st + row * 288 + c * 16));
        }
        publish(ws, WS_F3 + (unsigned)((r * 4 + w) * 16 + j) * 8, tag, lane);
        asm volatile("s_barrier" ::: "memory");
    }
    TL(8);
    lane = opq(lane0);
    final_tile(P, rs, tag, lds, lds0, 2 * r + (j >> 3), j & 7, w, lane);
    if (idx == 0 && w == 0) {
#pragma unroll 1
        for (int n = 0; n < 4; ++n) {
            await(ws, WS_F1 + (unsigned)(((4 * n + (lane >> 4)) * 4) * 16 + (lane & 15)) * 8, tag);
            await(ws, WS_F5 + (unsigned)((4 * n + (lane >> 4)) * 64 + 4 * (lane & 15)) * 8, tag);
        }
        if (lane == 0) stf(ws, WS_NONCE, tag);
    }
}

constexpr int AT_CONST = 3 * AT_CH;
DI void attn_issue(const char* kv, int ci, unsigned ldsb, int w, int lane) {
    const char* src = kv + (size_t)ci * AT_CH;
#pragma unroll
    for (int n = 0; n < 5; ++n) dma_now<false>(src, (unsigned)((n * 4 + w) * 1024 + lane * 16), ldsb + (n * 4 + w) * 1024);
    if (w == (ci & 3)) dma_now<false>(src, (unsigned)(20480 + lane * 16), ldsb + 20480);
}
#define ZERO16 (f32x16{0, 0, 0, 0, 0, 0, 0, 0, 0, 0, 0, 0, 0, 0, 0, 0})
#define BF8(x) __builtin_bit_cast(bf16x8, x)
#define GRP(S, o_, Pv, j_) do { SB(); unsigned pa_, pb_; float t0_, t1_, t2_, t3_; \
    asm volatile("v_exp_f32 %2, %6\n\tv_exp_f32 %3, %7\n\tv_exp_f32 %4, %8\n\tv_exp_f32 %5, %9\n\tv_cvt_pk_bf16_f32 %0, %2, %3\n\tv_cvt_pk_bf16_f32 %1, %4, %5" \
        : "=&v"(pa_), "=&v"(pb_), "=&v"(t0_), "=&v"(t1_), "=&v"(t2_), "=&v"(t3_) : "v"(S[o_]), "v"(S[o_ + 1]), "v"(S[o_ + 2]), "v"(S[o_ + 3])); \
    Pv[j_] = pa_; Pv[j_ + 1] = pb_; SB(); } while (0)
DI void attn_step(f32x16 (&o)[2], f32x16& sA, u32x4& p1, bf16x8& kf1, bf16x8& kf2, bf16x8& vf1, const bf16x8 (&qf1)[2], const u32x4 (&qf2)[2],
                  lptr& pv, unsigned vstep, lptr pk1n  , lptr pk2n  ) {
    f32x16 sB; u32x4 p0;
    sB = MFMA32(kf1, qf1[1], ZERO16);
    const bf16x8 vf0 = *(LAS bf16x8*)(pv); pv += vstep;      GRP(sA, 0, p0, 0);
    o[1] = MFMA32(vf1, BF8(p1), o[1]);
    const bf16x8 nk1 = *(LAS bf16x8*)(pk1n);                 GRP(sA, 4, p0, 2);
    sB = MFMA32(kf2, BF8(qf2[1]), sB);
    const bf16x8 nv1 = *(LAS bf16x8*)(pv); pv += vstep;      GRP(sA, 8, p1, 0);
    o[0] = MFMA32(vf0, BF8(p0), o[0]);
    const bf16x8 nk2 = *(LAS bf16x8*)(pk2n);                 GRP(sA, 12, p1, 2);
    sA = MFMA32(nk1, qf1[0], ZERO16);                        GRP(sB, 0, p0, 0);
    o[0] = MFMA32(nv1, BF8(p1), o[0]);                       GRP(sB, 4, p0, 2);
    sA = MFMA32(nk2, BF8(qf2[0]), sA);                       GRP(sB, 8, p1, 0);
    o[1] = MFMA32(vf0, BF8(p0), o[1]);                       GRP(sB, 12, p1, 2);
    kf1 = nk1; kf2 = nk2; vf1 = nv1;
}
struct Prefetch {
    const char* ws; const char* zq;
    const char* wq;
    unsigned f2; u64 tag, seen;
    int k;
    unsigned lds0;
    DI void step(int ci, int w, int lane) {
        if (ci < 4 && w < 2) touch(lds0, wq + (size_t)(lane & 15) * (SEQ * 2) + (ci * 8 + w * 4 + (lane >> 4)) * 128);
        if (k < 0) {
            if (__builtin_amdgcn_readfirstlane(__all(seen == tag))) k = 0; else seen = ldf(ws, f2);
        }
        if (k >= 0 && k < 32) {
            const int kk = k + lane / 9;
            touch(lds0, zq + (size_t)(kk < 31 ? kk : 31) * 18432 + (lane % 9) * 128);
            k += (k == 0) ? 7 : 5;
        }
    }
};
template <bool PF>
DI void attn_sweep(f32x16 (&o)[2], const bf16x8 (&qf1)[2], const u32x4 (&qf2)[2], const char* kv, lptr lds, unsigned lds0, int w, int lane, Prefetch& pf) {
    const int row = lane & 31, hh = lane >> 5;
    const lptr pk1 = lds + lane * 16, pk2 = lds + AT_K2 + row * 16;
    const unsigned vmul = row < 18 ? 1u : 0u, vstep = vmul * 576u;
    const lptr vbase = row < 18 ? lds + AT_V + hh * 288 + row * 16 : lds + AT_CONST + (row == 18 ? 0 : 16);
    wait_bar<5>();
    bf16x8 kf1 = *(LAS bf16x8*)(pk1), kf2 = *(LAS bf16x8*)(pk2), vf1 = kf1;
    f32x16 sA = MFMA32(kf1, qf1[0], ZERO16);
    sA = MFMA32(kf2, BF8(qf2[0]), sA);
    asm volatile("s_nop 7\n\ts_nop 7" : "+v"(sA));
    u32x4 p1 = {0u, 0u, 0u, 0u};
#pragma unroll
    for (int b = 0; b < 2; ++b) o[b] = ZERO16;
    unsigned cur = 0;
    for (int ci = 0; ci < 8; ++ci) {
        const unsigned nxt = (cur == 2 * AT_CH) ? 0u : cur + AT_CH;
        lptr pv = vbase + vmul * cur;
#pragma unroll
        for (int kb = 0; kb < 7; ++kb) attn_step(o, sA, p1, kf1, kf2, vf1, qf1, qf2, pv, vstep, pk1 + cur + (kb + 1) * 1024, pk2 + cur + (kb + 1) * 512);
        wait_bar<0>();
        if (ci + 2 < 8) attn_issue(kv, ci + 2, lds0 + (nxt == 2 * AT_CH ? 0u : nxt + AT_CH), w, lane);
        if (PF) pf.step(ci + 1, w, lane);
        const unsigned kn = (ci + 1 < 8) ? nxt : cur;
        attn_step(o, sA, p1, kf1, kf2, vf1, qf1, qf2, pv, vstep, pk1 + kn, pk2 + kn);
        cur = nxt;
    }
    asm volatile("s_nop 1" : "+v"(p1));
    o[1] = MFMA32(vf1, BF8(p1), o[1]);
    asm volatile("" : "+v"(o[0]), "+v"(o[1]), "+v"(sA));
}
DI void attn_rowmax(float (&mx)[2], const bf16x8 (&qf1)[2], const u32x4 (&qf2)[2], const char* kv, lptr lds, unsigned lds0, int w, int lane) {
    mx[0] = -INFINITY; mx[1] = -INFINITY;
#pragma unroll 1
    for (int ci = 0; ci < 8; ++ci) {
        WAIT_BAR0();
        attn_issue(kv, ci, lds0, w, lane);
        WAIT_BAR0();
#pragma unroll 1
        for (int kb = 0; kb < 8; ++kb) {
            const bf16x8 kf1 = *(LAS bf16x8*)(lds + lane * 16 + kb * 1024), kf2 = *(LAS bf16x8*)(lds + AT_K2 + (lane & 31) * 16 + kb * 512);
#pragma unroll
            for (int b = 0; b < 2; ++b) {
                f32x16 s = MFMA32(kf1, qf1[b], ZERO16);
                s = MFMA32(kf2, BF8(qf2[b]), s);
#pragma unroll
                for (int e = 0; e < 16; ++e) mx[b] = fmaxf(mx[b], s[e]);
            }
        }
    }
    WAIT_BAR0();
}
DI void role_a(const Params& P, u64 tag, lptr lds, unsigned lds0, int idx, int w, int lane) {
    const int x = idx & 7, y = idx >> 3;
    char* ws = P.ws;
    const __amdgpu_buffer_rsrc_t rs = ws_rsrc(ws);
    const int row = lane & 31, hh = lane >> 5;
    {
        const int r = (x >> 1) * 4 + (y >> 3), c = (x & 1) * 8 + (y & 7), g = lane >> 4;
        const int col0 = (108 * c) & ~7, shift = 108 * c - col0;
        Gemm<7, true> G; G.init(lds, lds0, w, lane);
        G.set_a(lane, 1728 * 2);
        b_offsets_rowmajor<4>(G.boff, 32 * w, lane, DM * 2);
        constexpr size_t QSUB = (size_t)32 * 1728 * 2;
        const char* wq = (const char*)P.wqkv + (size_t)col0 * 2;
        const char* xb = (const char*)P.ax + (size_t)r * 128 * DM * 2;
        G.pro_start(wq, wq + QSUB, wq + 2 * QSUB, xb, xb + 128);
        G.pro_finish();
        TL(1);
        f32x4 acc[7][2];
        zero_acc<7>(acc);
        {
            const char* a = wq + 3 * QSUB; const char* b = xb + 128;
            gemm_steady<0>(G, acc, a, QSUB, b, 0, 7);
            G.template kstep<1, 1>(acc, b, a, nullptr, nullptr);
            G.template kstep<2, 0>(acc, nullptr, nullptr, nullptr, nullptr);
        }
        TL(2);
        {
            const int t = threadIdx.x, rq = (r & 3) * 144;
#pragma unroll
            for (int n = 0; n < 3; ++n) {
                const int id = n * 256 + t, row = id < 576 ? id >> 2 : 0, o = (id & 3) == 3 ? 284 : (id & 3) * 128;
                touch(lds0, (const char*)P.win + (size_t)(rq + row) * (4608 * 2) + (144 * c) * 2 + o);
            }
        }
        asm volatile("s_waitcnt lgkmcnt(0)\n\ts_barrier" ::: "memory");
        constexpr int TP = 232;
        constexpr float QS = 0.23570226039551584f * 1.4426950408889634f;
        const lptr T = lds + w * (32 * TP);
#pragma unroll
        for (int i = 0; i < 7; ++i) {
            float sc[4];
#pragma unroll
            for (int e = 0; e < 4; ++e) sc[e] = (col0 + 16 * i + 4 * g + e < 576) ? QS : 1.0f;
#pragma unroll
            for (int tt = 0; tt < 2; ++tt) {
                u32x2 v; v[0] = pk2(acc[i][tt][0] * sc[0], acc[i][tt][1] * sc[1]); v[1] = pk2(acc[i][tt][2] * sc[2], acc[i][tt][3] * sc[3]);
                *(LAS u32x2*)(T + (16 * tt + (lane & 15)) * TP + (16 * i + 4 * g) * 2) = v;
            }
        }
        LGKM0();
        const int blk = 4 * r + w;
#pragma unroll 1
        for (int uu = 0; uu < 6; ++uu) {
            const int u = 6 * c + uu, type = u >> 5, h = u & 31, n0 = shift + 18 * uu;
            const unsigned kvb = WS_KV + (unsigned)(h * 8 + (blk >> 3)) * AT_CH;
            if (type < 2) {
                const lptr src = T + row * TP + (n0 + 8 * hh) * 2;
                u32x4 v;
#pragma unroll
                for (int e = 0; e < 4; ++e) v[e] = *(LAS unsigned*)(src + 4 * e);
                const unsigned ex = *(LAS unsigned*)(T + row * TP + (n0 + 16) * 2);
                if (type == 0) {
                    st16(rs, WS_QF1 + (unsigned)(h * 64 + blk) * 1024 + lane * 16, v);
                    if (hh == 0) st4(rs, WS_Q2 + (unsigned)(h * SEQ + blk * 32 + row) * 4, ex);
                } else {
                    st16(rs, kvb + (blk & 7) * 1024 + lane * 16, v);
                    if (hh == 0) st16(rs, kvb + AT_K2 + ((blk & 7) * 32 + row) * 16, u32x4{ex, 0x3f803f80u, 0x00003f80u, 0u});
                    float s = 0.f;
#pragma unroll
                    for (int e = 0; e < 4; ++e) s += bflo(v[e]) * bflo(v[e]) + bfhi(v[e]) * bfhi(v[e]);
                    if (hh == 0) s += bflo(ex) * bflo(ex) + bfhi(ex) * bfhi(ex);
                    s = red32(s);
#pragma unroll
                    for (int o = 16; o >= 1; o >>= 1) s = fmaxf(s, __shfl_xor(s, o));
                    if (lane == 0) st4(rs, WS_KMAX + (unsigned)(h * 64 + blk) * 4, __float_as_uint(s));
                }
            } else if (row < 18) {
#pragma unroll
                for (int s = 0; s < 2; ++s) {
                    u32x4 v;
#pragma unroll
                    for (int e = 0; e < 4; ++e) {
                        const int k0 = 16 * s + 8 * (e >> 1) + 4 * hh + 2 * (e & 1);
                        const unsigned lo = *(LAS u16*)(T + k0 * TP + (n0 + row) * 2), hi = *(LAS u16*)(T + (k0 + 1) * TP + (n0 + row) * 2);
                        v[e] = lo | (hi << 16);
                    }
                    st16(rs, kvb + AT_V + (blk & 7) * 1152 + s * 576 + hh * 288 + row * 16, v);
                }
            }
        }
        publish(ws, WS_F5 + (unsigned)(c * 64 + blk) * 8, tag, lane);
        TL(3);
    }
    const int h = x * 4 + (y >> 3), qc = y & 7;
    const char* kv = ws + WS_KV + (size_t)h * 8 * AT_CH;
    if (w == 0) {
        await(ws, WS_F5 + (unsigned)((h / 6) * 64 + lane) * 8, tag);
        await(ws, WS_F5 + (unsigned)(((32 + h) / 6) * 64 + lane) * 8, tag);
        await(ws, WS_F5 + (unsigned)(((64 + h) / 6) * 64 + lane) * 8, tag);
        acquire();
    }
    if (threadIdx.x < 2) { const unsigned v = threadIdx.x == 0 ? 0x3f803f80u : 0u; *(LAS u32x4*)(lds + AT_CONST + threadIdx.x * 16) = u32x4{v, v, v, v}; }
    WAIT_BAR0();
    TL(4);
    attn_issue(kv, 0, lds0, w, lane);
    attn_issue(kv, 1, lds0 + AT_CH, w, lane);
    float kmax2 = __uint_as_float(ld4(rs, WS_KMAX + (unsigned)(h * 64 + lane) * 4));
#pragma unroll
    for (int o = 32; o >= 1; o >>= 1) kmax2 = fmaxf(kmax2, __shfl_xor(kmax2, o));
    bf16x8 qf1[2]; u32x4 qf2[2];
#pragma unroll
    for (int b = 0; b < 2; ++b) {
        const int qb = qc * 8 + w * 2 + b;
        const u32x4 v = ld16(rs, WS_QF1 + (unsigned)(h * 64 + qb) * 1024 + lane * 16);
        const unsigned ex = ld4(rs, WS_Q2 + (unsigned)(h * SEQ + qb * 32 + row) * 4);
        float s = 0.f;
#pragma unroll
        for (int e = 0; e < 4; ++e) s += bflo(v[e]) * bflo(v[e]) + bfhi(v[e]) * bfhi(v[e]);
        if (hh == 0) s += bflo(ex) * bflo(ex) + bfhi(ex) * bfhi(ex);
        s = red32(s);
        const float M = sqrtf(s * kmax2) * 1.0078125f;
        const unsigned mb = ((__float_as_uint(M) + 0xffffu) >> 16) | 0x8000u;
        qf1[b] = __builtin_bit_cast(bf16x8, v);
        qf2[b] = hh == 0 ? u32x4{ex, mb, 0u, 0u} : u32x4{0u, 0u, 0u, 0u};
    }
    f32x16 o[2];
    TL(5); TLC(12);
    Prefetch pf;
    {
        const int r = (x >> 1) * 4 + (y >> 3), j = (x & 1) * 8 + (y & 7);
        pf.ws = ws; pf.zq = ws + WS_Z2N + (size_t)j * 64 * 9216 + ((r & 3) * 36 + w * 9) * 128;
        pf.wq = (const char*)P.wsp + (size_t)(r * 128 + (j & 7) * 16) * (SEQ * 2);
        pf.f2 = WS_F2 + (unsigned)(j * 64 + lane) * 8; pf.tag = tag; pf.seen = tag + 1; pf.lds0 = lds0;
        pf.k = -1;
        pf.step(0, w, lane);
    }
    attn_sweep<true>(o, qf1, qf2, kv, lds, lds0, w, lane, pf);
    TL(6); TLC(13);
    {
        bool bad = false;
#pragma unroll
        for (int b = 0; b < 2; ++b) { const float l = __shfl(o[b][10], row); bad |= !(l >= 8.271806125530277e-25f); }
        LAS unsigned* vote = (LAS unsigned*)(lds + AT_CONST + 32);
        if (threadIdx.x == 0) *vote = 0u;
        WAIT_BAR0();
        if (__any(bad) && lane == 0) *vote = 1u;
        WAIT_BAR0();
        if (__builtin_amdgcn_readfirstlane(*vote) != 0u) {
#pragma unroll
            for (int b = 0; b < 2; ++b) qf2[b][1] = 0u;
            float mx[2];
            attn_rowmax(mx, qf1, qf2, kv, lds, lds0, w, lane);
#pragma unroll
            for (int b = 0; b < 2; ++b) {
                const float nm = -max32(mx[b]);
                const unsigned h1 = __float_as_uint(nm) & 0xffff0000u; const float r1 = nm - __uint_as_float(h1);
                const unsigned h2 = __float_as_uint(r1) & 0xffff0000u; const float r2 = r1 - __uint_as_float(h2);
                const unsigned h3 = __float_as_uint(r2) & 0xffff0000u;
                if (hh == 0) { qf2[b][1] = (h1 >> 16) | h2; qf2[b][2] = h3 >> 16; }
            }
            attn_issue(kv, 0, lds0, w, lane);
            attn_issue(kv, 1, lds0 + AT_CH, w, lane);
            attn_sweep<false>(o, qf1, qf2, kv, lds, lds0, w, lane, pf);
            WAIT_BAR0();
        }
    }
    const lptr st = lds + w * 2560;
#pragma unroll
    for (int b = 0; b < 2; ++b) {
        const float inv = 1.0f / __shfl(o[b][10], row);
        const lptr rp = st + (32 * b + row) * 36 + hh * 8;
        *(LAS unsigned*)(rp + 0) = pk2(o[b][0] * inv, o[b][1] * inv);
        *(LAS unsigned*)(rp + 4) = pk2(o[b][2] * inv, o[b][3] * inv);
        *(LAS unsigned*)(rp + 16) = pk2(o[b][4] * inv, o[b][5] * inv);
        *(LAS unsigned*)(rp + 20) = pk2(o[b][6] * inv, o[b][7] * inv);
        if (hh == 0) *(LAS unsigned*)(rp + 32) = pk2(o[b][8] * inv, o[b][9] * inv);
    }
    LGKM0();
#pragma unroll
    for (int n = 0; n < 9; ++n) {
        const int id = n * 64 + lane, rr = id / 9, cc = id - rr * 9;
        st4(rs, WS_O + (unsigned)(qc * 256 + w * 64 + rr) * (DM * 2) + h * 36 + cc * 4, *(LAS unsigned*)(st + id * 4));
    }
    publish(ws, WS_F4 + (unsigned)((qc * 4 + w) * 32 + h) * 8, tag, lane);
    TL(7);
    VM0();
#pragma unroll 1
    for (int n = 0; n < 64 && pf.k < 32; ++n) {
        pf.step(8, w, lane);
        VM0();
        __builtin_amdgcn_s_sleep(32);
    }
    {
        const int t = threadIdx.x;
        const int r = (x >> 1) * 4 + (y >> 3), j = (x & 1) * 8 + (y & 7), tb = 2 * r + (j >> 3);
#define WLINE(id_) (576 * ((id_) / 18) + 18 * y + (id_) % 18)
#define WTOUCH(n_) do { const int id = (n_) * 256 + t, ln = WLINE(id < 810 ? id : 0); touch(lds0, ln < 20736 ? (const char*)P.wout + (size_t)ln * 128 : (const char*)P.wo + (size_t)(ln - 20736) * 128); } while (0)
        await<16>(ws, WS_F3 + (unsigned)((r * 4 + 2 * (tb & 1)) * 16 + (lane & 31)) * 8, tag);
        WTOUCH(0);
        const char* ub = ws + WS_U + (size_t)tb * 36 * 8192 + (size_t)(j & 7) * 8 * 128;
        touch_sc1(lds0, ub + (size_t)(t >> 3) * 8192 + (t & 7) * 128);
        touch_sc1(lds0, ub + (size_t)(32 + ((t >> 3) & 3)) * 8192 + (t & 7) * 128);
        WTOUCH(1);
        VM0();
        __builtin_amdgcn_s_sleep(64);
        WTOUCH(2); WTOUCH(3);
        VM0();
    }
    TL(8);
}

__global__ __launch_bounds__(256, 2) void gated_mix_kernel(Params P) {
    extern __shared__ __attribute__((aligned(16))) char smem[];
    const lptr lds = (lptr)smem;
    const unsigned lds0 = (unsigned)(uintptr_t)lds;
    const int w = __builtin_amdgcn_readfirstlane(threadIdx.x >> 6), lane = threadIdx.x & 63;
    TL(0); TLC(14);
    const u64 tag = ldf(P.ws, WS_NONCE) + 1ull;
    if (blockIdx.x < 256) role_g(P, tag, lds, lds0, blockIdx.x, w, lane);
    else role_a(P, tag, lds, lds0, blockIdx.x - 256, w, lane);
    TL(11); TLC(15);
}

extern "C" void kernel_launch(void* const* d_in, const int* in_sizes, int n_in, void* d_out, int out_size, void* d_ws, size_t ws_size, hipStream_t stream) {
    Params P;
    P.ax = (const u16*)d_in[0]; P.wqkv = (const u16*)d_in[1]; P.wo = (const u16*)d_in[2]; P.win = (const u16*)d_in[4];
    P.wsp = (const u16*)d_in[5]; P.bsp = (const u16*)d_in[6]; P.wout = (const u16*)d_in[7]; P.gw = (const u16*)d_in[8];
    P.out = (u16*)d_out; P.ws = (char*)d_ws;
    (void)hipFuncSetAttribute((const void*)gated_mix_kernel, hipFuncAttributeMaxDynamicSharedMemorySize, LDS_BYTES);
    gated_mix_kernel<<<512, 256, LDS_BYTES, stream>>>(P);
}
```

```cpp
#include <hip/hip_runtime.h>
#include <stdint.h>

typedef unsigned short u16;
typedef unsigned long long u64;
typedef short bf16x8 __attribute__((ext_vector_type(8)));
typedef short s16x4 __attribute__((ext_vector_type(4)));
typedef float f32x2 __attribute__((ext_vector_type(2)));
typedef float f32x4 __attribute__((ext_vector_type(4)));
typedef float f32x16 __attribute__((ext_vector_type(16)));
typedef unsigned u32x2 __attribute__((ext_vector_type(2)));
typedef unsigned u32x4 __attribute__((ext_vector_type(4)));
typedef __bf16 bf16x2_t __attribute__((ext_vector_type(2)));
#define LAS __attribute__((address_space(3)))
#define GAS __attribute__((address_space(1)))
typedef LAS char* lptr;
typedef GAS u64* gu64;
#define DI __device__ __forceinline__

constexpr int SEQ = 2048, DM = 576, FH = 2304;
#define PRIO_G1 1
#define EXP_REP 1

constexpr unsigned WS_Z2N = 0;
constexpr unsigned WS_U = WS_Z2N + 9437184;
constexpr unsigned WS_QF1 = WS_U + 9437184;
constexpr unsigned WS_Q2 = WS_QF1 + 2097152;
constexpr unsigned AT_CH = 21504, AT_K2 = 8192, AT_V = 12288;
constexpr unsigned WS_KV = WS_Q2 + 262144;
constexpr unsigned WS_KMAX = WS_KV + 32 * 8 * AT_CH;
constexpr unsigned WS_O = WS_KMAX + 8192;
constexpr unsigned WS_GATE = WS_O + 2359296;
constexpr unsigned WS_XBUF = WS_GATE + 8192;
constexpr unsigned WS_F1 = WS_XBUF + 262144;
constexpr unsigned WS_F2 = WS_F1 + 8192;
constexpr unsigned WS_F3 = WS_F2 + 8192;
constexpr unsigned WS_F4 = WS_F3 + 8192;
constexpr unsigned WS_F5 = WS_F4 + 8192;
constexpr unsigned WS_NONCE = WS_F5 + 8192;
constexpr unsigned WS_END = WS_NONCE + 256;

struct Params {
    const u16* ax; const u16* wqkv; const u16* wo; const u16* win; const u16* wsp;
    const u16* bsp; const u16* wout; const u16* gw; u16* out; char* ws;
};

DI unsigned pk2(float lo, float hi) { f32x2 v = {lo, hi}; return __builtin_bit_cast(unsigned, __builtin_convertvector(v, bf16x2_t)); }
DI float bflo(unsigned u) { return __uint_as_float(u << 16); }
DI float bfhi(unsigned u) { return __uint_as_float(u & 0xffff0000u); }
DI float red16(float x) { auto r = __builtin_amdgcn_permlane16_swap(__float_as_uint(x), __float_as_uint(x), false, false); return __uint_as_float(r[0]) + __uint_as_float(r[1]); }
DI float red32(float x) { auto r = __builtin_amdgcn_permlane32_swap(__float_as_uint(x), __float_as_uint(x), false, false); return __uint_as_float(r[0]) + __uint_as_float(r[1]); }
DI float max32(float x) { auto r = __builtin_amdgcn_permlane32_swap(__float_as_uint(x), __float_as_uint(x), false, false); return fmaxf(__uint_as_float(r[0]), __uint_as_float(r[1])); }
DI float gelu_tanh(float x) {
    constexpr float C0 = -2.0f * 0.7978845608028654f * 1.4426950408889634f, C1 = C0 * 0.044715f;
    const float t = x * x, p = __builtin_fmaf(t, C1, C0), e = __builtin_amdgcn_exp2f(x * p);
    return x * __builtin_amdgcn_rcpf(1.0f + e);
}
#define SB() __builtin_amdgcn_sched_barrier(0)
DI int opq(int x) { asm volatile("" : "+v"(x)); return x; }
#define WAIT_BAR0() asm volatile("s_waitcnt vmcnt(0) lgkmcnt(0)\n\ts_barrier" ::: "memory")
#define LGKM0() asm volatile("s_waitcnt lgkmcnt(0)" ::: "memory")
#define VM0() asm volatile("s_waitcnt vmcnt(0)" ::: "memory")
template <int N> DI void wait_bar() {
    asm volatile("" ::: "memory");
    __builtin_amdgcn_s_waitcnt((N & 15) | ((N >> 4) << 14) | (7 << 4));
    __builtin_amdgcn_s_barrier();
    asm volatile("" ::: "memory");
}
DI s16x4 trread(lptr p) { return __builtin_amdgcn_ds_read_tr16_b64_v4i16((LAS s16x4*)p); }
#define MFMA16(a, b, c) __builtin_amdgcn_mfma_f32_16x16x32_bf16((a), (b), (c), 0, 0, 0)
#define MFMA32(a, b, c) __builtin_amdgcn_mfma_f32_32x32x16_bf16((a), (b), (c), 0, 0, 0)
DI void m0_set(unsigned base, int imm) { asm volatile("s_add_u32 m0, %0, %1" :: "s"(base), "i"(imm) : "scc", "memory"); }
template <bool SC1> DI void dma(const void* sbase, unsigned voff) {
    if constexpr (SC1) asm volatile("global_load_lds_dwordx4 %0, %1 sc1" :: "v"(voff), "s"(sbase) : "memory");
    else asm volatile("global_load_lds_dwordx4 %0, %1" :: "v"(voff), "s"(sbase) : "memory");
}
template <bool SC1> DI void dma_now(const void* sbase, unsigned voff, unsigned lds_addr) { m0_set(lds_addr, 0); asm volatile("s_nop 0"); dma<SC1>(sbase, voff); }

DI __amdgpu_buffer_rsrc_t ws_rsrc(char* ws) { return __builtin_amdgcn_make_buffer_rsrc(ws, 0, WS_END, 0x00020000); }
DI void st16(__amdgpu_buffer_rsrc_t rs, unsigned off, u32x4 v) { __builtin_amdgcn_raw_buffer_store_b128(v, rs, off, 0, 16); }
DI void st4(__amdgpu_buffer_rsrc_t rs, unsigned off, unsigned v) { __builtin_amdgcn_raw_buffer_store_b32(v, rs, off, 0, 16); }
DI u32x4 ld16(__amdgpu_buffer_rsrc_t rs, unsigned off) { return __builtin_amdgcn_raw_buffer_load_b128(rs, off, 0, 16); }
DI unsigned ld4(__amdgpu_buffer_rsrc_t rs, unsigned off) { return __builtin_amdgcn_raw_buffer_load_b32(rs, off, 0, 16); }
DI u64 ldf(const char* ws, unsigned off) { return __hip_atomic_load((gu64)(ws + off), __ATOMIC_RELAXED, __HIP_MEMORY_SCOPE_AGENT); }
DI void stf(char* ws, unsigned off, u64 v) { __hip_atomic_store((gu64)(ws + off), v, __ATOMIC_RELAXED, __HIP_MEMORY_SCOPE_AGENT); }
#define DUMPADDR 81664
constexpr int LDS_DUMP = DUMPADDR;
DI void touch(unsigned lds0, const char* p) { m0_set(lds0, LDS_DUMP); asm volatile("s_nop 0\n\tglobal_load_lds_dword %0, off" :: "v"(p) : "memory"); }
DI void touch_sc1(unsigned lds0, const char* p) { m0_set(lds0, LDS_DUMP); asm volatile("s_nop 0\n\tglobal_load_lds_dword %0, off sc1" :: "v"(p) : "memory"); }
DI void publish(char* ws, unsigned flag_off, u64 tag, int lane) { VM0(); if (lane == 0) stf(ws, flag_off, tag); }
template <int SLEEP = 8> DI void await(const char* ws, unsigned flag_off, u64 tag) {
    for (unsigned spins = 0; spins < (1u << 16); ++spins) {
        if (__all(ldf(ws, flag_off) == tag)) break;
        __builtin_amdgcn_s_sleep(SLEEP);
    }
    __builtin_amdgcn_fence(__ATOMIC_ACQUIRE, "wavefront");
}

#define TL(n) do {} while (0)
#define TLC(n) do {} while (0)

DI int rho2k(int rho) { const int r = rho & 31; return (rho & 32) + 8 * ((r >> 2) & 3) + 4 * (r >> 4) + (r & 3); }

constexpr int LDS_BYTES = 81920;
constexpr int A_SLOT = 9216, B_RING = 4 * A_SLOT, B_SLOT = 16384;
template <int NI> DI void b_offsets_rowmajor(unsigned (&off)[NI], int rw, int lane, unsigned ldb) {
#pragma unroll
    for (int n = 0; n < NI; ++n) { const int row = rw + 8 * n + (lane >> 3); off[n] = (unsigned)row * ldb + (unsigned)(((lane & 7) ^ (row & 7)) * 16); }
}
template <int NF, bool ATR, bool SWAP = false> struct Gemm {
    static constexpr int P = NF * 32, NCH = NF * 2;
    static constexpr int NBASE = (NF * 2 * 32 / 64) / 4;
    static constexpr int NS = NBASE + 2;
    unsigned aoff[NBASE], aext, boff[4];
    lptr pa, pb0, pb1;
    unsigned dA, dAx, dB;
    int w;
    bf16x8 af[NF], bc[2];
    DI void init(lptr lds, unsigned lds0, int w_, int lane) {
        const int g = lane >> 4, q = (lane & 15) >> 2, p = lane & 3;
        w = w_;
        pa = ATR ? lds + (4 * g + q) * P + 8 * p : lds + lane * 16;
        pb0 = lds + B_RING + (32 * w + (lane & 15)) * 128 + ((g ^ (lane & 7)) * 16);
        pb1 = lds + B_RING + (32 * w + (lane & 15)) * 128 + (((4 + g) ^ (lane & 7)) * 16);
        dA = lds0 + w * 1024; dB = lds0 + B_RING + w * 4096;
        dAx = lds0 + NBASE * 4096 + (NF == 9 ? 0 : w * 1024);
    }
    DI void set_a(int lane, unsigned ldb) {
#pragma unroll
        for (int n = 0; n <= NBASE; ++n) {
            const int id = n < NBASE ? (n * 4 + w) * 64 + lane : NBASE * 256 + (NF == 9 ? 0 : (w < 3 ? w : 0) * 64) + lane;
            unsigned o;
            if constexpr (ATR) { const int rho = id / NCH, c = id - rho * NCH; o = (unsigned)rho2k(rho) * ldb + (unsigned)c * 16u; } else o = (unsigned)id * 16u;
            if (n < NBASE) aoff[n] = o; else aext = o;
        }
    }
    DI bool has_extra(int slot_idx) const { return NF == 9 ? w == slot_idx : w < 3; }
    DI bf16x8 read_a(lptr p, int off, int i) const {
        if constexpr (ATR) { const s16x4 lo = trread(p + off + 32 * i), hi = trread(p + off + 16 * P + 32 * i); return __builtin_shufflevector(lo, hi, 0, 1, 2, 3, 4, 5, 6, 7); }
        else return *(LAS bf16x8*)(p + off + 1024 * i);
    }
    DI void issue_a(const char* src, int si) const {
#pragma unroll
        for (int n = 0; n < NBASE; ++n) { m0_set(dA, si * A_SLOT + n * 4096); asm volatile("s_nop 0"); dma<!ATR>(src, aoff[n]); }
        if (has_extra(si)) { m0_set(dAx, si * A_SLOT); asm volatile("s_nop 0"); dma<!ATR>(src, aext); }
    }
    DI void issue_b(const char* src, int si, int n0) const {
#pragma unroll
        for (int n = n0; n < n0 + 2; ++n) { m0_set(dB, si * B_SLOT + n * 1024); asm volatile("s_nop 0"); dma<false>(src, boff[n]); }
    }
    DI void pro_start(const char* a0, const char* a1, const char* a2, const char* b0, const char* b1) const {
        issue_b(b0, 0, 0); issue_a(a0, 0); issue_b(b0, 0, 2); issue_a(a1, 1); issue_b(b1, 1, 0); issue_a(a2, 2);
    }
    DI void pro_finish() {
        wait_bar<2 * NBASE + 2>();
#pragma unroll
        for (int i = 0; i < NF - 2; ++i) af[i] = read_a(pa, 0, i);
        bc[0] = *(LAS bf16x8*)(pb0); bc[1] = *(LAS bf16x8*)(pb0 + 2048);
    }
    template <int SC, int SBI, int NWAIT, int NMID, bool DOB, bool DOA>
    DI void substep(f32x4 (&acc)[NF][2], const char* srcB, const char* srcA) {
        constexpr bool ODD = (SC & 1) != 0;
        constexpr int SN = (SC + 1) & 3, ST = (SC + 3) & 3;
        constexpr int B0 = ODD ? 0 : 2, BT = SBI ^ (ODD ? 0 : 1);
        constexpr int STEP = NF == 9 ? 2 : 1;
        wait_bar<NWAIT>();
        const lptr pbn = ODD ? pb0 + (SBI ^ 1) * B_SLOT : pb1 + SBI * B_SLOT;
        af[NF - 2] = read_a(pa, SC * A_SLOT, NF - 2);
        af[NF - 1] = read_a(pa, SC * A_SLOT, NF - 1);
        bf16x8 bn[2];
#pragma unroll
        for (int i = 0; i < NF; ++i) {
            const int d = (i % STEP == 0) ? i / STEP : -1;
            const bool isB = DOB && d >= 0 && d < 2, isA = DOA && d >= 2 && d < 2 + NBASE;
            if (isB) m0_set(dB, BT * B_SLOT + (B0 + d) * 1024);
            if (isA) m0_set(dA, ST * A_SLOT + (d - 2) * 4096);
            if (i == 1) bn[0] = *(LAS bf16x8*)(pbn);
            SB();
            acc[i][0] = SWAP ? MFMA16(bc[0], af[i], acc[i][0]) : MFMA16(af[i], bc[0], acc[i][0]);
            SB();
            if (isB) dma<false>(srcB, boff[B0 + d]);
            if (isA) dma<!ATR>(srcA, aoff[d - 2]);
            SB();
            acc[i][1] = SWAP ? MFMA16(bc[1], af[i], acc[i][1]) : MFMA16(af[i], bc[1], acc[i][1]);
            SB();
            if (i < NF - 2) af[i] = read_a(pa, SN * A_SLOT, i);
            if (i == NF - 2) {
                if (NMID >= 0) asm volatile("s_waitcnt vmcnt(%0)" :: "n"(NMID < 0 ? 0 : NMID) : "memory");
                bn[1] = *(LAS bf16x8*)(pbn + 2048);
            }
            SB();
        }
        if (DOA) { if (has_extra(ST)) { m0_set(dAx, ST * A_SLOT); asm volatile("s_nop 0"); dma<!ATR>(srcA, aext); } }
        bc[0] = bn[0]; bc[1] = bn[1];
    }
    template <int MODE, int PAR, int XT = 0>
    DI void kstep(f32x4 (&acc)[NF][2], const char* bE, const char* aE, const char* bO, const char* aO) {
        substep<2 * PAR, PAR, MODE == 2 ? 0 : NS + XT, -1, MODE != 2, MODE != 2>(acc, bE, aE);
        substep<2 * PAR + 1, PAR, MODE == 2 ? 0 : NS + XT, MODE == 0 ? NBASE + NS : (MODE == 1 ? NBASE : -1), MODE == 0, MODE == 0>(acc, bO, aO);
    }
};
template <int PAR0, int NF, bool ATR, bool SWAP>
DI void gemm_steady(Gemm<NF, ATR, SWAP>& G, f32x4 (&acc)[NF][2], const char*& a, size_t asub, const char*& b, int k0, int k1) {
    int k = k0;
    if (PAR0 == 1 && k < k1) { G.template kstep<0, 1>(acc, b, a, b + 128, a + asub); a += 2 * asub; b += 128; ++k; }
    for (; k + 1 < k1; k += 2) {
        G.template kstep<0, 0>(acc, b, a, b + 128, a + asub); a += 2 * asub; b += 128;
        G.template kstep<0, 1>(acc, b, a, b + 128, a + asub); a += 2 * asub; b += 128;
    }
    if (k < k1) { G.template kstep<0, 0>(acc, b, a, b + 128, a + asub); a += 2 * asub; b += 128; }
}
template <int NF> DI void zero_acc(f32x4 (&acc)[NF][2]) {
#pragma unroll
    for (int i = 0; i < NF; ++i) { acc[i][0] = f32x4{0, 0, 0, 0}; acc[i][1] = f32x4{0, 0, 0, 0}; }
}

constexpr int F_AB = 64 * 144, F_STAGE = F_AB + 8192;
struct FFrag { bf16x8 a[5], b[2]; };
DI void fread(FFrag& f, lptr pa, lptr pb) {
    f.b[0] = *(LAS bf16x8*)(pb); f.b[1] = *(LAS bf16x8*)(pb + 2048);
#pragma unroll
    for (int i = 0; i < 5; ++i) { const s16x4 lo = trread(pa + 32 * i), hi = trread(pa + 16 * 144 + 32 * i); f.a[i] = __builtin_shufflevector(lo, hi, 0, 1, 2, 3, 4, 5, 6, 7); }
}
template <int SL, int NWAIT, bool DOI>
DI void fstep(f32x4 (&acc)[5][2], FFrag& f, lptr pa, lptr pb, const char* sa, const char* sb, unsigned dA, unsigned dB, unsigned dX, const unsigned (&aoff)[3], unsigned bo0, unsigned bo1, int w) {
    wait_bar<NWAIT>();
    constexpr int SN = (SL + 1) & 3;
    FFrag n;
#pragma unroll
    for (int i = 0; i < 5; ++i) {
        if (DOI && i < 2) m0_set(dA, SL * F_STAGE + i * 4096);
        if (DOI && i >= 2 && i < 4) m0_set(dB, SL * F_STAGE + (i - 2) * 1024);
        if (i < 2) n.b[i] = *(LAS bf16x8*)(pb + SN * F_STAGE + i * 2048);
        SB();
        acc[i][0] = MFMA16(f.a[i], f.b[0], acc[i][0]);
        SB();
        if (DOI && i < 2) dma<false>(sa, aoff[i]);
        if (DOI && i >= 2 && i < 4) dma<true>(sb, i == 2 ? bo0 : bo1);
        { const s16x4 lo = trread(pa + SN * F_STAGE + 32 * i), hi = trread(pa + SN * F_STAGE + 16 * 144 + 32 * i); n.a[i] = __builtin_shufflevector(lo, hi, 0, 1, 2, 3, 4, 5, 6, 7); }
        SB();
        acc[i][1] = MFMA16(f.a[i], f.b[1], acc[i][1]);
        SB();
    }
    if (DOI && w == SL) { m0_set(dX, SL * F_STAGE); asm volatile("s_nop 0"); dma<false>(sa, aoff[2]); }
    f = n;
}
struct FinalCtx { unsigned aoff[3]; };
DI void final_aoff(unsigned (&aoff)[3], int w, int lane) {
#pragma unroll
    for (int n = 0; n < 3; ++n) {
        const int id = n < 2 ? (n * 4 + w) * 64 + lane : 512 + lane, rho = id / 9, c = id - rho * 9;
        aoff[n] = (unsigned)rho2k(rho) * (DM * 2) + (unsigned)c * 16u;
    }
}
DI void final_weights(const Params& P, unsigned lds0, int e, int k, int w, const unsigned (&aoff)[3]) {
    const char* src = (const char*)P.wout + 72 * e * 2 + (size_t)k * 64 * DM * 2;
    dma_now<false>(src, aoff[0], lds0 + k * F_STAGE + w * 1024);
    dma_now<false>(src, aoff[1], lds0 + k * F_STAGE + (4 + w) * 1024);
    if (w == k) dma_now<false>(src, aoff[2], lds0 + k * F_STAGE + 8192);
}
DI void final_tile(const Params& P, __amdgpu_buffer_rsrc_t rs, u64 tag, lptr lds, unsigned lds0, int tb, int e, int w, int lane) {
    const int d0 = 72 * e;
    const int th = w & 1, kh = w >> 1;
    int g = lane >> 4; const int q = (lane & 15) >> 2, p = lane & 3;
    char* ws = P.ws;
    unsigned aoff[3], boffo[2];
    final_aoff(aoff, w, lane);
    const unsigned bu0 = (unsigned)(w * 2048 + lane * 16), bu1 = bu0 + 1024;
    b_offsets_rowmajor<2>(boffo, 16 * w, lane, DM * 2);
    const lptr pa = lds + (32 * kh + 4 * g + q) * 144 + 8 * p;
    const lptr pb = lds + F_AB + (32 * th + (lane & 15)) * 128 + (((4 * kh + g) ^ (lane & 7)) * 16);
    const unsigned dA = lds0 + w * 1024, dB = lds0 + F_AB + w * 2048, dX = lds0 + 8192;
    f32x4 accb[5][2], acca[5][2];
    zero_acc<5>(accb); zero_acc<5>(acca);
    const char* wout = (const char*)P.wout + d0 * 2;
    const char* wo = (const char*)P.wo + d0 * 2;
    const char* ub = ws + WS_U + (size_t)tb * 36 * 8192;
    const char* obp = ws + WS_O + (size_t)tb * 64 * DM * 2;
    final_weights(P, lds0, e, 2, w, aoff);
    final_weights(P, lds0, e, 3, w, aoff);
    if (w == 0) await<4>(ws, WS_F3 + (unsigned)(((tb >> 1) * 4 + 2 * (tb & 1)) * 16 + (lane & 31)) * 8, tag);
    asm volatile("s_barrier" ::: "memory");
#pragma unroll
    for (int k = 0; k < 4; ++k) {
        dma_now<true>(ub + k * 8192, bu0, dB + k * F_STAGE);
        dma_now<true>(ub + k * 8192, bu1, dB + k * F_STAGE + 1024);
    }
    TL(9);
    FFrag f;
    wait_bar<6>();
    fread(f, pa, pb);
#define FSRC(kn) ((kn) < 36 ? wout + (size_t)(kn) * 64 * DM * 2 : wo + (size_t)((kn) - 36) * 64 * DM * 2), ((kn) < 36 ? ub + (size_t)(kn) * 8192 : obp + ((kn) - 36) * 128)
#define FB0(kn) ((kn) < 36 ? bu0 : boffo[0]), ((kn) < 36 ? bu1 : boffo[1])
#define FSTEP(SL, NW, acc_, kn) fstep<SL, NW, true>(acc_, f, pa, pb, FSRC(kn), dA, dB, dX, aoff, FB0(kn), w)
    FSTEP(0, 4, accb, 4);
    FSTEP(1, 2 + 4, accb, 5);
    FSTEP(2, 8, accb, 6);
    FSTEP(3, 8, accb, 7);
    TLC(24); TL(26);
    for (int k = 4; k < 32; k += 4) {
        fstep<0, 8, true>(accb, f, pa, pb, wout + (size_t)(k + 4) * 64 * DM * 2, ub + (size_t)(k + 4) * 8192, dA, dB, dX, aoff, bu0, bu1, w);
        fstep<1, 8, true>(accb, f, pa, pb, wout + (size_t)(k + 5) * 64 * DM * 2, ub + (size_t)(k + 5) * 8192, dA, dB, dX, aoff, bu0, bu1, w);
        fstep<2, 8, true>(accb, f, pa, pb, wout + (size_t)(k + 6) * 64 * DM * 2, ub + (size_t)(k + 6) * 8192, dA, dB, dX, aoff, bu0, bu1, w);
        fstep<3, 8, true>(accb, f, pa, pb, wout + (size_t)(k + 7) * 64 * DM * 2, ub + (size_t)(k + 7) * 8192, dA, dB, dX, aoff, bu0, bu1, w);
    }
    TLC(25); TL(27);
    if (w == 0) await<4>(ws, WS_F4 + (unsigned)(tb * 32 + (lane & 31)) * 8, tag);
    asm volatile("s_barrier" ::: "memory");
    FSTEP(0, 8, accb, 36);
    FSTEP(1, 8, accb, 37);
    FSTEP(2, 8, accb, 38);
    FSTEP(3, 8, accb, 39);
    FSTEP(0, 8, acca, 40);
    FSTEP(1, 8, acca, 41);
    FSTEP(2, 8, acca, 42);
    FSTEP(3, 8, acca, 43);
    FSTEP(0, 8, acca, 44);
    fstep<1, 8, false>(acca, f, pa, pb, nullptr, nullptr, dA, dB, dX, aoff, 0, 0, w);
    fstep<2, 4, false>(acca, f, pa, pb, nullptr, nullptr, dA, dB, dX, aoff, 0, 0, w);
    fstep<3, 0, false>(acca, f, pa, pb, nullptr, nullptr, dA, dB, dX, aoff, 0, 0, w);
    fstep<0, 0, false>(acca, f, pa, pb, nullptr, nullptr, dA, dB, dX, aoff, 0, 0, w);
    TL(10);
    lane = opq(lane); g = lane >> 4;
    float gt[2];
#pragma unroll
    for (int tt = 0; tt < 2; ++tt) gt[tt] = __uint_as_float(ld4(rs, WS_GATE + (unsigned)(tb * 64 + 32 * th + 16 * tt + (lane & 15)) * 4));
#pragma unroll
    for (int i = 0; i < 5; ++i)
#pragma unroll
        for (int tt = 0; tt < 2; ++tt) accb[i][tt] = acca[i][tt] * gt[tt] + accb[i][tt] * (1.0f - gt[tt]);
    WAIT_BAR0();
    const lptr xch = lds + th * 10240 + lane * 16;
    if (kh == 1) {
#pragma unroll
        for (int i = 0; i < 5; ++i)
#pragma unroll
            for (int tt = 0; tt < 2; ++tt) *(LAS f32x4*)(xch + (i * 2 + tt) * 1024) = accb[i][tt];
    }
    WAIT_BAR0();
    const lptr st = lds + 20480;
    if (kh == 0) {
#pragma unroll
        for (int i = 0; i < 5; ++i)
#pragma unroll
            for (int tt = 0; tt < 2; ++tt) {
                const f32x4 v = accb[i][tt] + *(LAS f32x4*)(xch + (i * 2 + tt) * 1024);
                u32x2 o; o[0] = pk2(v[0], v[1]); o[1] = pk2(v[2], v[3]);
                *(LAS u32x2*)(st + (32 * th + 16 * tt + (lane & 15)) * 160 + (16 * i + 4 * g) * 2) = o;
            }
    }
    WAIT_BAR0();
    for (int id = threadIdx.x; id < 64 * 9; id += 256) {
        const int row = id / 9, c = id - row * 9;
        *(u32x4*)((char*)P.out + (size_t)(tb * 64 + row) * (DM * 2) + d0 * 2 + c * 16) = *(LAS u32x4*)(st + row * 160 + c * 16);
    }
}

DI void role_g(const Params& P, u64 tag, lptr lds, unsigned lds0, int idx, int w, int lane0) {
    __builtin_amdgcn_s_setprio(PRIO_G1);
    const int x = idx & 7, y = idx >> 3;
    const int r = (x >> 1) * 4 + (y >> 3), j = (x & 1) * 8 + (y & 7);
    int lane = lane0;
    char* ws = P.ws;
    const __amdgpu_buffer_rsrc_t rs = ws_rsrc(ws);
    f32x4 acc[9][2];
    u32x4 z1[9];
    {
        Gemm<9, true> G; G.init(lds, lds0, w, lane);
        G.set_a(lane, 4608 * 2);
        b_offsets_rowmajor<4>(G.boff, 32 * w, lane, DM * 2);
        constexpr size_t WSUB = (size_t)32 * 4608 * 2;
        const char* wz2 = (const char*)P.win + (size_t)(FH + 144 * j) * 2;
        const char* wz1 = (const char*)P.win + (size_t)(144 * j) * 2;
        const char* xb = (const char*)P.ax + (size_t)r * 128 * DM * 2;
        G.pro_start(wz2, wz2 + WSUB, wz2 + 2 * WSUB, xb, xb + 128);
        G.pro_finish();
        TL(1);
        zero_acc<9>(acc);
        {
            const char* a = wz2 + 3 * WSUB; const char* b = xb + 128;
            gemm_steady<0>(G, acc, a, WSUB, b, 0, 7);
            G.template kstep<0, 1>(acc, xb + 8 * 128, wz2 + 17 * WSUB, xb, wz1);
            G.template kstep<0, 0>(acc, xb, wz1 + WSUB, xb + 128, wz1 + 2 * WSUB);
        }
        TL(2);
        lane = opq(lane0);
        u32x4 zp[9];
        {
            float mean[2], m2[2];
#pragma unroll
            for (int tt = 0; tt < 2; ++tt) {
                float s = 0.f;
#pragma unroll
                for (int i = 0; i < 9; ++i)
#pragma unroll
                    for (int e = 0; e < 4; ++e) { acc[i][tt][e] = gelu_tanh(acc[i][tt][e]); s += acc[i][tt][e]; }
                s = red32(red16(s));
                mean[tt] = s * (1.0f / 144.0f);
                float qq = 0.f;
#pragma unroll
                for (int i = 0; i < 9; ++i) {
#pragma unroll
                    for (int e = 0; e < 4; ++e) { const float d = acc[i][tt][e] - mean[tt]; qq = __builtin_fmaf(d, d, qq); }
                }
                m2[tt] = red32(red16(qq));
            }
            if (lane < 32) {
                const int tt = lane >> 4;
                const float mu = tt ? mean[1] : mean[0], mm = tt ? m2[1] : m2[0];
                stf(ws, WS_XBUF + (unsigned)((r * 16 + j) * 128 + 32 * w + lane) * 8, ((u64)__float_as_uint(mm) << 32) | __float_as_uint(mu));
            }
#pragma unroll
            for (int i = 0; i < 9; ++i)
                zp[i] = u32x4{pk2(acc[i][0][0], acc[i][0][1]), pk2(acc[i][0][2], acc[i][0][3]), pk2(acc[i][1][0], acc[i][1][1]), pk2(acc[i][1][2], acc[i][1][3])};
            publish(ws, WS_F1 + (unsigned)((r * 4 + w) * 16 + j) * 8, tag, lane);
        }
        TL(3);
        await<4>(ws, WS_F1 + (unsigned)((r * 4 + w) * 16 + (lane & 15)) * 8, tag);
        TL(16);
        {
            const int g = lane >> 4;
            const unsigned xo = WS_XBUF + (unsigned)((r * 16 + (lane >> 5) * 8) * 128 + 32 * w + (lane & 31)) * 8;
            float m[8], q2[8], sm = 0.f;
#pragma unroll
            for (int e = 0; e < 8; ++e) { const u64 v = ldf(ws, xo + e * 1024); m[e] = __uint_as_float((unsigned)v); q2[e] = __uint_as_float((unsigned)(v >> 32)); sm += m[e]; }
            const float mean = red32(sm) * (1.0f / 16.0f);
            float qq = 0.f;
#pragma unroll
            for (int e = 0; e < 8; ++e) { const float d = m[e] - mean; qq += q2[e] + 144.0f * d * d; }
            qq = red32(qq);
            const float rstd = 1.0f / sqrtf(qq * (1.0f / 2304.0f) + 1e-5f);
            float ra[2], rb[2];
#pragma unroll
            for (int tt = 0; tt < 2; ++tt) { ra[tt] = __shfl(rstd, 16 * tt + (lane & 15)); rb[tt] = -__shfl(mean, 16 * tt + (lane & 15)) * ra[tt]; }
            const lptr st = lds + 69632 + w * 3072;
            const lptr rd = st + (4 * g + ((lane & 15) >> 2)) * 96 + 8 * (lane & 3);
            const unsigned dst = WS_Z2N + (unsigned)(j * 64 + 4 * r + w) * 9216 + lane * 16;
#pragma unroll
            for (int p3 = 0; p3 < 3; ++p3) {
#pragma unroll
                for (int tt = 0; tt < 2; ++tt) {
                    const int gp = 2 * tt + ((lane & 15) >> 3), h = (lane >> 2) & 1, ii = lane & 3;
                    const lptr rowp = st + (16 * h + 4 * gp + ii) * 96 + 8 * g;
#pragma unroll
                    for (int c3 = 0; c3 < 3; ++c3) {
                        const int i = 3 * p3 + c3;
                        u32x2 v;
                        v[0] = pk2(__builtin_fmaf(bflo(zp[i][2 * tt]), ra[tt], rb[tt]), __builtin_fmaf(bfhi(zp[i][2 * tt]), ra[tt], rb[tt]));
                        v[1] = pk2(__builtin_fmaf(bflo(zp[i][2 * tt + 1]), ra[tt], rb[tt]), __builtin_fmaf(bfhi(zp[i][2 * tt + 1]), ra[tt], rb[tt]));
                        *(LAS u32x2*)(rowp + 32 * c3) = v;
                    }
                }
                LGKM0();
#pragma unroll
                for (int c3 = 0; c3 < 3; ++c3) {
                    const s16x4 lo = trread(rd + 32 * c3), hi = trread(rd + 16 * 96 + 32 * c3);
                    st16(rs, dst + (3 * p3 + c3) * 1024, __builtin_bit_cast(u32x4, __builtin_shufflevector(lo, hi, 0, 1, 2, 3, 4, 5, 6, 7)));
                }
                LGKM0();
            }
        }
        TL(17);
#define ZK(XT, kk) G.template kstep<0, ((kk) + 9) & 1, XT>(acc, xb + ((kk) + 1) * 128, wz1 + (2 * (kk) + 3) * WSUB, xb + ((kk) + 2) * 128, wz1 + (2 * (kk) + 4) * WSUB)
        zero_acc<9>(acc);
        ZK(9, 0);
        ZK(0, 1);
        lane = opq(lane0);
        if (lane == 0) stf(ws, WS_F2 + (unsigned)(j * 64 + 4 * r + w) * 8, tag);
        TL(18);
        ZK(1, 2); ZK(0, 3); ZK(0, 4); ZK(0, 5); ZK(0, 6);
        G.template kstep<1, 0>(acc, xb + 8 * 128, wz1 + 17 * WSUB, nullptr, nullptr);
        G.template kstep<2, 1>(acc, nullptr, nullptr, nullptr, nullptr);
        TL(4);
    }
#pragma unroll
    for (int i = 0; i < 9; ++i)
        z1[i] = u32x4{pk2(gelu_tanh(acc[i][0][0]), gelu_tanh(acc[i][0][1])), pk2(gelu_tanh(acc[i][0][2]), gelu_tanh(acc[i][0][3])),
                      pk2(gelu_tanh(acc[i][1][0]), gelu_tanh(acc[i][1][1])), pk2(gelu_tanh(acc[i][1][2]), gelu_tanh(acc[i][1][3]))};
    TL(5);
    u16 bsv[2];
#pragma unroll
    for (int tt = 0; tt < 2; ++tt) bsv[tt] = P.bsp[r * 128 + 32 * w + 16 * tt + (lane & 15)];
    {
        Gemm<9, false> G; G.init(lds, lds0, w, lane);
        G.set_a(lane, 0);
        b_offsets_rowmajor<4>(G.boff, 32 * w, lane, SEQ * 2);
        const char* zb = ws + WS_Z2N + (size_t)j * 64 * 9216;
        const char* wb = (const char*)P.wsp + (size_t)r * 128 * SEQ * 2;
        if (w == 0) await<4>(ws, WS_F2 + (unsigned)(j * 64 + lane) * 8, tag);
        WAIT_BAR0();
        TL(6);
        __builtin_amdgcn_s_setprio(0);
        for (int rep = 0; rep < EXP_REP; ++rep) {
        if (rep) { WAIT_BAR0(); TL(6); }
        G.pro_start(zb, zb + 9216, zb + 2 * 9216, wb, wb + 128);
        G.pro_finish();
        zero_acc<9>(acc);
        TLC(12);
        const char* a = zb + 3 * 9216; const char* b = wb + 128;
        gemm_steady<0>(G, acc, a, (size_t)9216, b, 0, 30);
        G.template kstep<1, 0>(acc, b, a, nullptr, nullptr);
        G.template kstep<2, 1>(acc, nullptr, nullptr, nullptr, nullptr);
        TLC(13);
        }
    }
    TL(7);
    lane = opq(lane0);
    const int tbf = 2 * r + (j >> 3);
    {
        const int g = lane >> 4;
        float bs[2];
#pragma unroll
        for (int tt = 0; tt < 2; ++tt) bs[tt] = __uint_as_float((unsigned)bsv[tt] << 16);
        WAIT_BAR0();
        {
            unsigned fa[3];
            final_aoff(fa, w, lane);
            final_weights(P, lds0, j & 7, 0, w, fa);
            final_weights(P, lds0, j & 7, 1, w, fa);
        }
        const lptr st = lds + 45056 + w * 9216;
#pragma unroll
        for (int i = 0; i < 9; ++i)
#pragma unroll
            for (int tt = 0; tt < 2; ++tt) {
                u32x2 v;
                v[0] = pk2(bflo(z1[i][2 * tt]) * (acc[i][tt][0] + bs[tt]), bfhi(z1[i][2 * tt]) * (acc[i][tt][1] + bs[tt]));
                v[1] = pk2(bflo(z1[i][2 * tt + 1]) * (acc[i][tt][2] + bs[tt]), bfhi(z1[i][2 * tt + 1]) * (acc[i][tt][3] + bs[tt]));
                *(LAS u32x2*)(st + (16 * tt + (lane & 15)) * 288 + (16 * i + 4 * g) * 2) = v;
            }
        LGKM0();
        const int tb = 2 * r + (w >> 1);
#pragma unroll
        for (int n = 0; n < 9; ++n) {
            const int id = n * 64 + lane, row = id / 18, c = id - row * 18;
            const int cg = 18 * j + c, t64 = 32 * (w & 1) + row;
            st16(rs, WS_U + (unsigned)(tb * 36 + (cg >> 3)) * 8192 + t64 * 128 + (((cg & 7) ^ (t64 & 7)) * 16), *(LAS u32x4*)(st + row * 288 + c * 16));
        }
        publish(ws, WS_F3 + (unsigned)((r * 4 + w) * 16 + j) * 8, tag, lane);
        asm volatile("s_barrier" ::: "memory");
    }
    TL(8);
    lane = opq(lane0);
    final_tile(P, rs, tag, lds, lds0, tbf, j & 7, w, lane);
}

constexpr int AT_CONST = 3 * AT_CH;
DI void attn_issue(const char* kv, int ci, unsigned ldsb, int w, int lane) {
    const char* src = kv + (size_t)ci * AT_CH;
#pragma unroll
    for (int n = 0; n < 5; ++n) dma_now<true>(src, (unsigned)((n * 4 + w) * 1024 + lane * 16), ldsb + (n * 4 + w) * 1024);
    if (w == (ci & 3)) dma_now<true>(src, (unsigned)(20480 + lane * 16), ldsb + 20480);
}
#define ZERO16 (f32x16{0, 0, 0, 0, 0, 0, 0, 0, 0, 0, 0, 0, 0, 0, 0, 0})
#define BF8(x) __builtin_bit_cast(bf16x8, x)
#define GRP(S, o_, Pv, j_) do { SB(); unsigned pa_, pb_; float t0_, t1_, t2_, t3_; \
    asm volatile("v_exp_f32 %2, %6\n\tv_exp_f32 %3, %7\n\tv_exp_f32 %4, %8\n\tv_exp_f32 %5, %9\n\tv_cvt_pk_bf16_f32 %0, %2, %3\n\tv_cvt_pk_bf16_f32 %1, %4, %5" \
        : "=&v"(pa_), "=&v"(pb_), "=&v"(t0_), "=&v"(t1_), "=&v"(t2_), "=&v"(t3_) : "v"(S[o_]), "v"(S[o_ + 1]), "v"(S[o_ + 2]), "v"(S[o_ + 3])); \
    Pv[j_] = pa_; Pv[j_ + 1] = pb_; SB(); } while (0)
DI void attn_step(f32x16 (&o)[2], f32x16& sA, u32x4& p1, bf16x8& kf1, bf16x8& kf2, bf16x8& vf1, const bf16x8 (&qf1)[2], const u32x4 (&qf2)[2],
                  lptr& pv, unsigned vstep, lptr pk1n  , lptr pk2n  ) {
    f32x16 sB; u32x4 p0;
    sB = MFMA32(kf1, qf1[1], ZERO16);
    const bf16x8 vf0 = *(LAS bf16x8*)(pv); pv += vstep;      GRP(sA, 0, p0, 0);
    o[1] = MFMA32(vf1, BF8(p1), o[1]);
    const bf16x8 nk1 = *(LAS bf16x8*)(pk1n);                 GRP(sA, 4, p0, 2);
    sB = MFMA32(kf2, BF8(qf2[1]), sB);
    const bf16x8 nv1 = *(LAS bf16x8*)(pv); pv += vstep;      GRP(sA, 8, p1, 0);
    o[0] = MFMA32(vf0, BF8(p0), o[0]);
    const bf16x8 nk2 = *(LAS bf16x8*)(pk2n);                 GRP(sA, 12, p1, 2);
    sA = MFMA32(nk1, qf1[0], ZERO16);                        GRP(sB, 0, p0, 0);
    o[0] = MFMA32(nv1, BF8(p1), o[0]);                       GRP(sB, 4, p0, 2);
    sA = MFMA32(nk2, BF8(qf2[0]), sA);                       GRP(sB, 8, p1, 0);
    o[1] = MFMA32(vf0, BF8(p0), o[1]);                       GRP(sB, 12, p1, 2);
    kf1 = nk1; kf2 = nk2; vf1 = nv1;
}
struct Prefetch {
    const char* ws; const char* zq;
    const char* wq;
    unsigned f2; u64 tag, seen;
    int k;
    unsigned lds0;
    DI void step(int ci, int w, int lane) {
        if (ci < 4 && w < 2) touch(lds0, wq + (size_t)(lane & 15) * (SEQ * 2) + (ci * 8 + w * 4 + (lane >> 4)) * 128);
        if (k < 0) {
            if (__builtin_amdgcn_readfirstlane(__all(seen == tag))) k = 0; else seen = ldf(ws, f2);
        }
        if (k >= 0 && k < 32) {
            const int kk = k + lane / 9;
            touch_sc1(lds0, zq + (size_t)(kk < 31 ? kk : 31) * 18432 + (lane % 9) * 128);
            k += (k == 0) ? 7 : 5;
        }
    }
};
template <bool PF>
DI void attn_sweep(f32x16 (&o)[2], const bf16x8 (&qf1)[2], const u32x4 (&qf2)[2], const char* kv, lptr lds, unsigned lds0, int w, int lane, Prefetch& pf) {
    const int row = lane & 31, hh = lane >> 5;
    const lptr pk1 = lds + lane * 16, pk2 = lds + AT_K2 + row * 16;
    const unsigned vmul = row < 18 ? 1u : 0u, vstep = vmul * 576u;
    const lptr vbase = row < 18 ? lds + AT_V + hh * 288 + row * 16 : lds + AT_CONST + (row == 18 ? 0 : 16);
    wait_bar<5>();
    bf16x8 kf1 = *(LAS bf16x8*)(pk1), kf2 = *(LAS bf16x8*)(pk2), vf1 = kf1;
    f32x16 sA = MFMA32(kf1, qf1[0], ZERO16);
    sA = MFMA32(kf2, BF8(qf2[0]), sA);
    asm volatile("s_nop 7\n\ts_nop 7" : "+v"(sA));
    u32x4 p1 = {0u, 0u, 0u, 0u};
#pragma unroll
    for (int b = 0; b < 2; ++b) o[b] = ZERO16;
    unsigned cur = 0;
    for (int ci = 0; ci < 8; ++ci) {
        const unsigned nxt = (cur == 2 * AT_CH) ? 0u : cur + AT_CH;
        lptr pv = vbase + vmul * cur;
#pragma unroll
        for (int kb = 0; kb < 7; ++kb) attn_step(o, sA, p1, kf1, kf2, vf1, qf1, qf2, pv, vstep, pk1 + cur + (kb + 1) * 1024, pk2 + cur + (kb + 1) * 512);
        wait_bar<0>();
        if (ci + 2 < 8) attn_issue(kv, ci + 2, lds0 + (nxt == 2 * AT_CH ? 0u : nxt + AT_CH), w, lane);
        if (PF) pf.step(ci + 1, w, lane);
        const unsigned kn = (ci + 1 < 8) ? nxt : cur;
        attn_step(o, sA, p1, kf1, kf2, vf1, qf1, qf2, pv, vstep, pk1 + kn, pk2 + kn);
        cur = nxt;
    }
    asm volatile("s_nop 1" : "+v"(p1));
    o[1] = MFMA32(vf1, BF8(p1), o[1]);
    asm volatile("" : "+v"(o[0]), "+v"(o[1]), "+v"(sA));
}
DI void attn_rowmax(float (&mx)[2], const bf16x8 (&qf1)[2], const u32x4 (&qf2)[2], const char* kv, lptr lds, unsigned lds0, int w, int lane) {
    mx[0] = -INFINITY; mx[1] = -INFINITY;
#pragma unroll 1
    for (int ci = 0; ci < 8; ++ci) {
        WAIT_BAR0();
        attn_issue(kv, ci, lds0, w, lane);
        WAIT_BAR0();
#pragma unroll 1
        for (int kb = 0; kb < 8; ++kb) {
            const bf16x8 kf1 = *(LAS bf16x8*)(lds + lane * 16 + kb * 1024), kf2 = *(LAS bf16x8*)(lds + AT_K2 + (lane & 31) * 16 + kb * 512);
#pragma unroll
            for (int b = 0; b < 2; ++b) {
                f32x16 s = MFMA32(kf1, qf1[b], ZERO16);
                s = MFMA32(kf2, BF8(qf2[b]), s);
#pragma unroll
                for (int e = 0; e < 16; ++e) mx[b] = fmaxf(mx[b], s[e]);
            }
        }
    }
    WAIT_BAR0();
}
DI void role_a(const Params& P, u64 tag, lptr lds, unsigned lds0, int idx, int w, int lane) {
    const int x = idx & 7, y = idx >> 3;
    char* ws = P.ws;
    const __amdgpu_buffer_rsrc_t rs = ws_rsrc(ws);
    const int row = lane & 31, hh = lane >> 5;
    {
        const int r = (x >> 1) * 4 + (y >> 3), c = (x & 1) * 8 + (y & 7);
        const int col0 = (108 * c) & ~7, shift = 108 * c - col0;
        constexpr size_t QSUB = (size_t)32 * 1728 * 2;
        const char* wq = (const char*)P.wqkv + (size_t)col0 * 2;
        const char* xb = (const char*)P.ax + (size_t)r * 128 * DM * 2;
        const int blk = 4 * r + w;
        f32x4 acc[7][2];
        zero_acc<7>(acc);
#define TOUCH_Z1() do { const int t = threadIdx.x, rq = (r & 3) * 144; _Pragma("unroll") for (int n = 0; n < 3; ++n) { \
            const int id = n * 256 + t, row_ = id < 576 ? id >> 2 : 0, o = (id & 3) == 3 ? 284 : (id & 3) * 128; \
            touch(lds0, (const char*)P.win + (size_t)(rq + row_) * (4608 * 2) + (144 * c) * 2 + o); } } while (0)
#define QKV_LOOP(G) do { G.init(lds, lds0, w, lane); G.set_a(lane, 1728 * 2); b_offsets_rowmajor<4>(G.boff, 32 * w, lane, DM * 2); \
            G.pro_start(wq, wq + QSUB, wq + 2 * QSUB, xb, xb + 128); G.pro_finish(); TL(1); \
            const char* a_ = wq + 3 * QSUB; const char* b_ = xb + 128; \
            gemm_steady<0>(G, acc, a_, QSUB, b_, 0, 7); \
            G.template kstep<1, 1>(acc, b_, a_, nullptr, nullptr); \
            G.template kstep<2, 0>(acc, nullptr, nullptr, nullptr, nullptr); TL(2); \
            asm volatile("s_waitcnt lgkmcnt(0)\n\ts_barrier" ::: "memory"); } while (0)
        if (c >= 11) {
            Gemm<7, true, true> G;
            QKV_LOOP(G);
            TL(27);
            const int ln = opq(lane), g = ln >> 4;
            const lptr V = lds + w * 6912;
#pragma unroll
            for (int i = 0; i < 7; ++i) {
                const int n = 16 * i + (ln & 15) - shift, uu = n / 18, d = n - 18 * uu;
                if (n >= 0 && n < 108) {
#pragma unroll
                    for (int tt = 0; tt < 2; ++tt)
                        *(LAS u32x2*)(V + uu * 1152 + tt * 576 + (g & 1) * 288 + d * 16 + (g >> 1) * 8) = u32x2{pk2(acc[i][tt][0], acc[i][tt][1]), pk2(acc[i][tt][2], acc[i][tt][3])};
                }
            }
            LGKM0();
            TL(25); TL(26);
#pragma unroll
            for (int n = 0; n < 7; ++n) {
                const int id = n * 64 + ln, uu = id / 72, rest = id - uu * 72;
                if (id < 432) st16(rs, WS_KV + (unsigned)(((6 * c + uu) & 31) * 8 + (blk >> 3)) * AT_CH + AT_V + (blk & 7) * 1152 + rest * 16, *(LAS u32x4*)(V + id * 16));
            }
        } else {
            Gemm<7, true> G;
            QKV_LOOP(G);
            TL(27);
            const int ln = opq(lane), g = ln >> 4, row = ln & 31, hh = ln >> 5;
            constexpr int TP = 232;
            constexpr float QS = 0.23570226039551584f * 1.4426950408889634f;
            const lptr T = lds + w * (32 * TP);
#pragma unroll
            for (int i = 0; i < 7; ++i) {
                float sc[4];
#pragma unroll
                for (int e = 0; e < 4; ++e) sc[e] = (col0 + 16 * i + 4 * g + e < 576) ? QS : 1.0f;
#pragma unroll
                for (int tt = 0; tt < 2; ++tt) {
                    u32x2 v; v[0] = pk2(acc[i][tt][0] * sc[0], acc[i][tt][1] * sc[1]); v[1] = pk2(acc[i][tt][2] * sc[2], acc[i][tt][3] * sc[3]);
                    *(LAS u32x2*)(T + (16 * tt + (ln & 15)) * TP + (16 * i + 4 * g) * 2) = v;
                }
            }
            LGKM0();
            TL(25);
            u32x4 fv[6]; unsigned fe[6];
#pragma unroll
            for (int uu = 0; uu < 6; ++uu) {
                const int n0 = shift + 18 * uu;
                const lptr src = T + row * TP + (n0 + 8 * hh) * 2;
#pragma unroll
                for (int e = 0; e < 4; ++e) fv[uu][e] = *(LAS unsigned*)(src + 4 * e);
                fe[uu] = *(LAS unsigned*)(T + row * TP + (n0 + 16) * 2);
            }
            asm volatile("" :: "v"(fv[5]), "v"(fe[5]));
            TL(26);
#pragma unroll
            for (int uu = 0; uu < 6; ++uu) {
                const int u = 6 * c + uu, type = u >> 5, h = u & 31, n0 = shift + 18 * uu;
                const unsigned kvb = WS_KV + (unsigned)(h * 8 + (blk >> 3)) * AT_CH;
                const u32x4 v = fv[uu]; const unsigned ex = fe[uu];
                if (type == 0) {
                    st16(rs, WS_QF1 + (unsigned)(h * 64 + blk) * 1024 + ln * 16, v);
                    if (hh == 0) st4(rs, WS_Q2 + (unsigned)(h * SEQ + blk * 32 + row) * 4, ex);
                } else if (type == 1) {
                    st16(rs, kvb + (blk & 7) * 1024 + ln * 16, v);
                    if (hh == 0) st16(rs, kvb + AT_K2 + ((blk & 7) * 32 + row) * 16, u32x4{ex, 0x3f803f80u, 0x00003f80u, 0u});
                    float s = 0.f;
#pragma unroll
                    for (int e = 0; e < 4; ++e) s += bflo(v[e]) * bflo(v[e]) + bfhi(v[e]) * bfhi(v[e]);
                    if (hh == 0) s += bflo(ex) * bflo(ex) + bfhi(ex) * bfhi(ex);
                    s = red32(s);
                    s = fmaxf(s, __builtin_bit_cast(float, __builtin_amdgcn_update_dpp(0, __builtin_bit_cast(int, s), 0xB1, 0xF, 0xF, true)));
                    s = fmaxf(s, __builtin_bit_cast(float, __builtin_amdgcn_update_dpp(0, __builtin_bit_cast(int, s), 0x4E, 0xF, 0xF, true)));
                    s = fmaxf(s, __builtin_bit_cast(float, __builtin_amdgcn_update_dpp(0, __builtin_bit_cast(int, s), 0x141, 0xF, 0xF, true)));
                    s = fmaxf(s, __builtin_bit_cast(float, __builtin_amdgcn_update_dpp(0, __builtin_bit_cast(int, s), 0x140, 0xF, 0xF, true)));
                    { auto rr = __builtin_amdgcn_permlane16_swap(__float_as_uint(s), __float_as_uint(s), false, false); s = fmaxf(__uint_as_float(rr[0]), __uint_as_float(rr[1])); }
                    if (ln == 0) st4(rs, WS_KMAX + (unsigned)(h * 64 + blk) * 4, __float_as_uint(s));
                } else if (row < 18) {
#pragma unroll
                    for (int s = 0; s < 2; ++s) {
                        u32x4 vv;
#pragma unroll
                        for (int e = 0; e < 4; ++e) {
                            const int k0 = 16 * s + 8 * (e >> 1) + 4 * hh + 2 * (e & 1);
                            const unsigned lo = *(LAS u16*)(T + k0 * TP + (n0 + row) * 2), hi = *(LAS u16*)(T + (k0 + 1) * TP + (n0 + row) * 2);
                            vv[e] = lo | (hi << 16);
                        }
                        st16(rs, kvb + AT_V + (blk & 7) * 1152 + s * 576 + hh * 288 + row * 16, vv);
                    }
                }
            }
        }
        TL(9);
        publish(ws, WS_F5 + (unsigned)(c * 64 + blk) * 8, tag, lane);
        TL(3);
        TOUCH_Z1();
    }
    const int h = x * 4 + (y >> 3), qc = y & 7;
    const char* kv = ws + WS_KV + (size_t)h * 8 * AT_CH;
    {
        const int grow = qc * 256 + w * 64 + 2 * h + hh;
        float s = 0.f;
#pragma unroll
        for (int n = 0; n < 3; ++n) {
            const int c = row + 32 * n;
            if (c < 72) {
                const u32x4 xv = *(const u32x4*)(P.ax + (size_t)grow * DM + c * 8), gv = *(const u32x4*)(P.gw + c * 8);
#pragma unroll
                for (int e = 0; e < 4; ++e) s += bflo(xv[e]) * bflo(gv[e]) + bfhi(xv[e]) * bfhi(gv[e]);
            }
        }
#pragma unroll
        for (int o = 16; o >= 1; o >>= 1) s += __shfl_xor(s, o);
        if (row == 0) st4(rs, WS_GATE + grow * 4, __float_as_uint(__builtin_amdgcn_rcpf(1.0f + __builtin_amdgcn_exp2f(-1.4426950408889634f * s))));
    }
    if (w == 0) {
        const unsigned fq = WS_F5 + (unsigned)((h / 6) * 64 + lane) * 8, fk = WS_F5 + (unsigned)(((32 + h) / 6) * 64 + lane) * 8, fv = WS_F5 + (unsigned)(((64 + h) / 6) * 64 + lane) * 8;
        for (unsigned spins = 0; spins < (1u << 16); ++spins) {
            const u64 a = ldf(ws, fq), b = ldf(ws, fk), c = ldf(ws, fv);
            if (__all(a == tag && b == tag && c == tag)) break;
            __builtin_amdgcn_s_sleep(6);
        }
    }
    if (threadIdx.x < 2) { const unsigned v = threadIdx.x == 0 ? 0x3f803f80u : 0u; *(LAS u32x4*)(lds + AT_CONST + threadIdx.x * 16) = u32x4{v, v, v, v}; }
    asm volatile("s_waitcnt lgkmcnt(0)\n\ts_barrier" ::: "memory");
    TL(4);
    const unsigned kmraw = ld4(rs, WS_KMAX + (unsigned)(h * 64 + lane) * 4);
    u32x4 qv[2]; unsigned qe[2];
#pragma unroll
    for (int b = 0; b < 2; ++b) {
        const int qb = qc * 8 + w * 2 + b;
        qv[b] = ld16(rs, WS_QF1 + (unsigned)(h * 64 + qb) * 1024 + lane * 16);
        qe[b] = ld4(rs, WS_Q2 + (unsigned)(h * SEQ + qb * 32 + row) * 4);
    }
    attn_issue(kv, 0, lds0, w, lane);
    attn_issue(kv, 1, lds0 + AT_CH, w, lane);
    TL(10);
    float kmax2 = __uint_as_float(kmraw);
#pragma unroll
    for (int o = 32; o >= 1; o >>= 1) kmax2 = fmaxf(kmax2, __shfl_xor(kmax2, o));
    bf16x8 qf1[2]; u32x4 qf2[2];
#pragma unroll
    for (int b = 0; b < 2; ++b) {
        const u32x4 v = qv[b];
        const unsigned ex = qe[b];
        float s = 0.f;
#pragma unroll
        for (int e = 0; e < 4; ++e) s += bflo(v[e]) * bflo(v[e]) + bfhi(v[e]) * bfhi(v[e]);
        if (hh == 0) s += bflo(ex) * bflo(ex) + bfhi(ex) * bfhi(ex);
        s = red32(s);
        const float M = sqrtf(s * kmax2) * 1.0078125f;
        const unsigned mb = ((__float_as_uint(M) + 0xffffu) >> 16) | 0x8000u;
        qf1[b] = __builtin_bit_cast(bf16x8, v);
        qf2[b] = hh == 0 ? u32x4{ex, mb, 0u, 0u} : u32x4{0u, 0u, 0u, 0u};
    }
    f32x16 o[2];
    TL(5); TLC(12);
    Prefetch pf;
    {
        const int r = (x >> 1) * 4 + (y >> 3), j = (x & 1) * 8 + (y & 7);
        pf.ws = ws; pf.zq = ws + WS_Z2N + (size_t)j * 64 * 9216 + ((r & 3) * 36 + w * 9) * 128;
        pf.wq = (const char*)P.wsp + (size_t)(r * 128 + (j & 7) * 16) * (SEQ * 2);
        pf.f2 = WS_F2 + (unsigned)(j * 64 + lane) * 8; pf.tag = tag; pf.seen = tag + 1; pf.lds0 = lds0;
        pf.k = -1;
        pf.step(0, w, lane);
    }
    attn_sweep<true>(o, qf1, qf2, kv, lds, lds0, w, lane, pf);
    TL(6); TLC(13);
    {
        bool bad = false;
#pragma unroll
        for (int b = 0; b < 2; ++b) { const float l = __shfl(o[b][10], row); bad |= !(l >= 8.271806125530277e-25f); }
        LAS unsigned* vote = (LAS unsigned*)(lds + AT_CONST + 32);
        if (threadIdx.x == 0) *vote = 0u;
        WAIT_BAR0();
        if (__any(bad) && lane == 0) *vote = 1u;
        WAIT_BAR0();
        if (__builtin_amdgcn_readfirstlane(*vote) != 0u) {
#pragma unroll
            for (int b = 0; b < 2; ++b) qf2[b][1] = 0u;
            float mx[2];
            attn_rowmax(mx, qf1, qf2, kv, lds, lds0, w, lane);
#pragma unroll
            for (int b = 0; b < 2; ++b) {
                const float nm = -max32(mx[b]);
                const unsigned h1 = __float_as_uint(nm) & 0xffff0000u; const float r1 = nm - __uint_as_float(h1);
                const unsigned h2 = __float_as_uint(r1) & 0xffff0000u; const float r2 = r1 - __uint_as_float(h2);
                const unsigned h3 = __float_as_uint(r2) & 0xffff0000u;
                if (hh == 0) { qf2[b][1] = (h1 >> 16) | h2; qf2[b][2] = h3 >> 16; }
            }
            attn_issue(kv, 0, lds0, w, lane);
            attn_issue(kv, 1, lds0 + AT_CH, w, lane);
            attn_sweep<false>(o, qf1, qf2, kv, lds, lds0, w, lane, pf);
            WAIT_BAR0();
        }
    }
    const lptr st = lds + w * 2560;
#pragma unroll
    for (int b = 0; b < 2; ++b) {
        const float inv = 1.0f / __shfl(o[b][10], row);
        const lptr rp = st + (32 * b + row) * 36 + hh * 8;
        *(LAS unsigned*)(rp + 0) = pk2(o[b][0] * inv, o[b][1] * inv);
        *(LAS unsigned*)(rp + 4) = pk2(o[b][2] * inv, o[b][3] * inv);
        *(LAS unsigned*)(rp + 16) = pk2(o[b][4] * inv, o[b][5] * inv);
        *(LAS unsigned*)(rp + 20) = pk2(o[b][6] * inv, o[b][7] * inv);
        if (hh == 0) *(LAS unsigned*)(rp + 32) = pk2(o[b][8] * inv, o[b][9] * inv);
    }
    LGKM0();
#pragma unroll
    for (int n = 0; n < 9; ++n) {
        const int id = n * 64 + lane, rr = id / 9, cc = id - rr * 9;
        st4(rs, WS_O + (unsigned)(qc * 256 + w * 64 + rr) * (DM * 2) + h * 36 + cc * 4, *(LAS unsigned*)(st + id * 4));
    }
    publish(ws, WS_F4 + (unsigned)((qc * 4 + w) * 32 + h) * 8, tag, lane);
    TL(7);
    if (idx == 0 && w == 0) {
#pragma unroll 1
        for (int n = 0; n < 4; ++n) {
            await(ws, WS_F1 + (unsigned)(((4 * n + (lane >> 4)) * 4) * 16 + (lane & 15)) * 8, tag);
            await(ws, WS_F5 + (unsigned)((4 * n + (lane >> 4)) * 64 + 4 * (lane & 15)) * 8, tag);
        }
        if (lane == 0) stf(ws, WS_NONCE, tag);
    }
    VM0();
#pragma unroll 1
    for (int n = 0; n < 64 && pf.k < 32; ++n) {
        pf.step(8, w, lane);
        VM0();
        __builtin_amdgcn_s_sleep(32);
    }
    {
        const int t = threadIdx.x;
        const int r = (x >> 1) * 4 + (y >> 3), j = (x & 1) * 8 + (y & 7), tb = 2 * r + (j >> 3);
#define WTOUCH(id_) do { const int id = (id_) < 810 ? (id_) : 0, ks = id / 18, ln = 18 * y + id % 18; \
            touch(lds0, ks < 36 ? (const char*)P.wout + (size_t)(576 * ks + ln) * 128 : (const char*)P.wo + (size_t)(576 * (ks - 36) + ln) * 128); } while (0)
        await<16>(ws, WS_F3 + (unsigned)((r * 4 + 2 * (tb & 1)) * 16 + (lane & 31)) * 8, tag);
        WTOUCH(t);
        const char* ub = ws + WS_U + (size_t)tb * 36 * 8192 + (size_t)(j & 7) * 8 * 128;
        touch_sc1(lds0, ub + (size_t)(t >> 3) * 8192 + (t & 7) * 128);
        touch_sc1(lds0, ub + (size_t)(32 + ((t >> 3) & 3)) * 8192 + (t & 7) * 128);
        WTOUCH(256 + t);
        VM0();
        __builtin_amdgcn_s_sleep(48);
        WTOUCH(512 + t); WTOUCH(768 + t);
        await<16>(ws, WS_F4 + (unsigned)(tb * 32 + (lane & 31)) * 8, tag);
        { const int id = (t < 72 ? t : 0) + (j & 7) * 72; touch_sc1(lds0, ws + WS_O + (size_t)(tb * 64 + id / 9) * (DM * 2) + (id % 9) * 128); }
        VM0();
    }
    TL(8);
}

__global__ __launch_bounds__(256, 2) void gated_mix_kernel(Params P) {
    extern __shared__ __attribute__((aligned(16))) char smem[];
    const lptr lds = (lptr)smem;
    const unsigned lds0 = (unsigned)(uintptr_t)lds;
    const int w = __builtin_amdgcn_readfirstlane(threadIdx.x >> 6), lane = threadIdx.x & 63;
    TL(0); TLC(14);
    const u64 tag = ldf(P.ws, WS_NONCE) + 1ull;
    if (blockIdx.x < 256) role_g(P, tag, lds, lds0, blockIdx.x, w, lane);
    else role_a(P, tag, lds, lds0, blockIdx.x - 256, w, lane);
    TL(11); TLC(15);
}

extern "C" void kernel_launch(void* const* d_in, const int* in_sizes, int n_in, void* d_out, int out_size, void* d_ws, size_t ws_size, hipStream_t stream) {
    Params P;
    P.ax = (const u16*)d_in[0]; P.wqkv = (const u16*)d_in[1]; P.wo = (const u16*)d_in[2]; P.win = (const u16*)d_in[4];
    P.wsp = (const u16*)d_in[5]; P.bsp = (const u16*)d_in[6]; P.wout = (const u16*)d_in[7]; P.gw = (const u16*)d_in[8];
    P.out = (u16*)d_out; P.ws = (char*)d_ws;
    (void)hipFuncSetAttribute((const void*)gated_mix_kernel, hipFuncAttributeMaxDynamicSharedMemorySize, LDS_BYTES);
    gated_mix_kernel<<<512, 256, LDS_BYTES, stream>>>(P);
}
```

```cpp
#include <hip/hip_runtime.h>
#include <stdint.h>

typedef unsigned short u16;
typedef unsigned long long u64;
typedef short bf16x8 __attribute__((ext_vector_type(8)));
typedef short s16x4 __attribute__((ext_vector_type(4)));
typedef float f32x2 __attribute__((ext_vector_type(2)));
typedef float f32x4 __attribute__((ext_vector_type(4)));
typedef float f32x16 __attribute__((ext_vector_type(16)));
typedef unsigned u32x2 __attribute__((ext_vector_type(2)));
typedef unsigned u32x4 __attribute__((ext_vector_type(4)));
typedef __bf16 bf16x2_t __attribute__((ext_vector_type(2)));
#define LAS __attribute__((address_space(3)))
#define GAS __attribute__((address_space(1)))
typedef LAS char* lptr;
typedef GAS u64* gu64;
#define DI __device__ __forceinline__

constexpr int SEQ = 2048, DM = 576, FH = 2304;
constexpr int CODE_BYTES = 100000;
#define PRIO_G1 1
#define EXP_REP 1

constexpr unsigned WS_Z2N = 0;
constexpr unsigned WS_U = WS_Z2N + 9437184;
constexpr unsigned WS_QF1 = WS_U + 9437184;
constexpr unsigned WS_Q2 = WS_QF1 + 2097152;
constexpr unsigned AT_CH = 21504, AT_K2 = 8192, AT_V = 12288;
constexpr unsigned WS_KV = WS_Q2 + 262144;
constexpr unsigned WS_KMAX = WS_KV + 32 * 8 * AT_CH;
constexpr unsigned WS_O = WS_KMAX + 8192;
constexpr unsigned WS_GATE = WS_O + 2359296;
constexpr unsigned WS_XBUF = WS_GATE + 8192;
constexpr unsigned WS_F1 = WS_XBUF + 262144;
constexpr unsigned WS_F2 = WS_F1 + 8192;
constexpr unsigned WS_F3 = WS_F2 + 8192;
constexpr unsigned WS_F4 = WS_F3 + 8192;
constexpr unsigned WS_F5 = WS_F4 + 8192;
constexpr unsigned WS_NONCE = WS_F5 + 8192;
constexpr unsigned WS_END = WS_NONCE + 256;

struct Params {
    const u16* ax; const u16* wqkv; const u16* wo; const u16* win; const u16* wsp;
    const u16* bsp; const u16* wout; const u16* gw; u16* out; char* ws;
};

DI unsigned pk2(float lo, float hi) { f32x2 v = {lo, hi}; return __builtin_bit_cast(unsigned, __builtin_convertvector(v, bf16x2_t)); }
DI float bflo(unsigned u) { return __uint_as_float(u << 16); }
DI float bfhi(unsigned u) { return __uint_as_float(u & 0xffff0000u); }
DI float red16(float x) { auto r = __builtin_amdgcn_permlane16_swap(__float_as_uint(x), __float_as_uint(x), false, false); return __uint_as_float(r[0]) + __uint_as_float(r[1]); }
DI float red32(float x) { auto r = __builtin_amdgcn_permlane32_swap(__float_as_uint(x), __float_as_uint(x), false, false); return __uint_as_float(r[0]) + __uint_as_float(r[1]); }
DI float max32(float x) { auto r = __builtin_amdgcn_permlane32_swap(__float_as_uint(x), __float_as_uint(x), false, false); return fmaxf(__uint_as_float(r[0]), __uint_as_float(r[1])); }
DI float gelu_tanh(float x) {
    constexpr float C0 = -2.0f * 0.7978845608028654f * 1.4426950408889634f, C1 = C0 * 0.044715f;
    const float t = x * x, p = __builtin_fmaf(t, C1, C0), e = __builtin_amdgcn_exp2f(x * p);
    return x * __builtin_amdgcn_rcpf(1.0f + e);
}
#define SB() __builtin_amdgcn_sched_barrier(0)
DI int opq(int x) { asm volatile("" : "+v"(x)); return x; }
#define WAIT_BAR0() asm volatile("s_waitcnt vmcnt(0) lgkmcnt(0)\n\ts_barrier" ::: "memory")
#define LGKM0() asm volatile("s_waitcnt lgkmcnt(0)" ::: "memory")
#define VM0() asm volatile("s_waitcnt vmcnt(0)" ::: "memory")
template <int N> DI void wait_bar() {
    asm volatile("" ::: "memory");
    __builtin_amdgcn_s_waitcnt((N & 15) | ((N >> 4) << 14) | (7 << 4));
    __builtin_amdgcn_s_barrier();
    asm volatile("" ::: "memory");
}
DI s16x4 trread(lptr p) { return __builtin_amdgcn_ds_read_tr16_b64_v4i16((LAS s16x4*)p); }
#define MFMA16(a, b, c) __builtin_amdgcn_mfma_f32_16x16x32_bf16((a), (b), (c), 0, 0, 0)
#define MFMA32(a, b, c) __builtin_amdgcn_mfma_f32_32x32x16_bf16((a), (b), (c), 0, 0, 0)
DI void m0_set(unsigned base, int imm) { asm volatile("s_add_u32 m0, %0, %1" :: "s"(base), "i"(imm) : "scc", "memory"); }
template <bool SC1> DI void dma(const void* sbase, unsigned voff) {
    if constexpr (SC1) asm volatile("global_load_lds_dwordx4 %0, %1 sc1" :: "v"(voff), "s"(sbase) : "memory");
    else asm volatile("global_load_lds_dwordx4 %0, %1" :: "v"(voff), "s"(sbase) : "memory");
}
template <bool SC1> DI void dma_now(const void* sbase, unsigned voff, unsigned lds_addr) { m0_set(lds_addr, 0); asm volatile("s_nop 0"); dma<SC1>(sbase, voff); }

DI __amdgpu_buffer_rsrc_t ws_rsrc(char* ws) { return __builtin_amdgcn_make_buffer_rsrc(ws, 0, WS_END, 0x00020000); }
DI void st16(__amdgpu_buffer_rsrc_t rs, unsigned off, u32x4 v) { __builtin_amdgcn_raw_buffer_store_b128(v, rs, off, 0, 16); }
DI void st4(__amdgpu_buffer_rsrc_t rs, unsigned off, unsigned v) { __builtin_amdgcn_raw_buffer_store_b32(v, rs, off, 0, 16); }
DI u32x4 ld16(__amdgpu_buffer_rsrc_t rs, unsigned off) { return __builtin_amdgcn_raw_buffer_load_b128(rs, off, 0, 16); }
DI unsigned ld4(__amdgpu_buffer_rsrc_t rs, unsigned off) { return __builtin_amdgcn_raw_buffer_load_b32(rs, off, 0, 16); }
DI u64 ldf(const char* ws, unsigned off) { return __hip_atomic_load((gu64)(ws + off), __ATOMIC_RELAXED, __HIP_MEMORY_SCOPE_AGENT); }
DI void stf(char* ws, unsigned off, u64 v) { __hip_atomic_store((gu64)(ws + off), v, __ATOMIC_RELAXED, __HIP_MEMORY_SCOPE_AGENT); }
#define DUMPADDR 81664
constexpr int LDS_DUMP = DUMPADDR;
DI void touch(unsigned lds0, const char* p) { m0_set(lds0, LDS_DUMP); asm volatile("s_nop 0\n\tglobal_load_lds_dword %0, off" :: "v"(p) : "memory"); }
DI void touch_sc1(unsigned lds0, const char* p) { m0_set(lds0, LDS_DUMP); asm volatile("s_nop 0\n\tglobal_load_lds_dword %0, off sc1" :: "v"(p) : "memory"); }
DI void publish(char* ws, unsigned flag_off, u64 tag, int lane) { VM0(); if (lane == 0) stf(ws, flag_off, tag); }
template <int SLEEP = 8> DI void await(const char* ws, unsigned flag_off, u64 tag) {
    for (unsigned spins = 0; spins < (1u << 16); ++spins) {
        if (__all(ldf(ws, flag_off) == tag)) break;
        __builtin_amdgcn_s_sleep(SLEEP);
    }
    __builtin_amdgcn_fence(__ATOMIC_ACQUIRE, "wavefront");
}

#define TL(n) do {} while (0)
#define TLC(n) do {} while (0)

DI int rho2k(int rho) { const int r = rho & 31; return (rho & 32) + 8 * ((r >> 2) & 3) + 4 * (r >> 4) + (r & 3); }

constexpr int LDS_BYTES = 81920;
constexpr int A_SLOT = 9216, B_RING = 4 * A_SLOT, B_SLOT = 16384;
template <int NI> DI void b_offsets_rowmajor(unsigned (&off)[NI], int rw, int lane, unsigned ldb) {
#pragma unroll
    for (int n = 0; n < NI; ++n) { const int row = rw + 8 * n + (lane >> 3); off[n] = (unsigned)row * ldb + (unsigned)(((lane & 7) ^ (row & 7)) * 16); }
}
template <int NF, bool ATR, bool SWAP = false> struct Gemm {
    static constexpr int P = NF * 32, NCH = NF * 2;
    static constexpr int NBASE = (NF * 2 * 32 / 64) / 4;
    static constexpr int NS = NBASE + 2;
    unsigned aoff[NBASE], aext, boff[4];
    lptr pa, pb0, pb1;
    unsigned dA, dAx, dB;
    int w;
    bf16x8 af[NF], bc[2];
    DI void init(lptr lds, unsigned lds0, int w_, int lane) {
        const int g = lane >> 4, q = (lane & 15) >> 2, p = lane & 3;
        w = w_;
        pa = ATR ? lds + (4 * g + q) * P + 8 * p : lds + lane * 16;
        pb0 = lds + B_RING + (32 * w + (lane & 15)) * 128 + ((g ^ (lane & 7)) * 16);
        pb1 = lds + B_RING + (32 * w + (lane & 15)) * 128 + (((4 + g) ^ (lane & 7)) * 16);
        dA = lds0 + w * 1024; dB = lds0 + B_RING + w * 4096;
        dAx = lds0 + NBASE * 4096 + (NF == 9 ? 0 : w * 1024);
    }
    DI void set_a(int lane, unsigned ldb) {
#pragma unroll
        for (int n = 0; n <= NBASE; ++n) {
            const int id = n < NBASE ? (n * 4 + w) * 64 + lane : NBASE * 256 + (NF == 9 ? 0 : (w < 3 ? w : 0) * 64) + lane;
            unsigned o;
            if constexpr (ATR) { const int rho = id / NCH, c = id - rho * NCH; o = (unsigned)rho2k(rho) * ldb + (unsigned)c * 16u; } else o = (unsigned)id * 16u;
            if (n < NBASE) aoff[n] = o; else aext = o;
        }
    }
    DI bool has_extra(int slot_idx) const { return NF == 9 ? w == slot_idx : w < 3; }
    DI bf16x8 read_a(lptr p, int off, int i) const {
        if constexpr (ATR) { const s16x4 lo = trread(p + off + 32 * i), hi = trread(p + off + 16 * P + 32 * i); return __builtin_shufflevector(lo, hi, 0, 1, 2, 3, 4, 5, 6, 7); }
        else return *(LAS bf16x8*)(p + off + 1024 * i);
    }
    DI void issue_a(const char* src, int si) const {
#pragma unroll
        for (int n = 0; n < NBASE; ++n) { m0_set(dA, si * A_SLOT + n * 4096); asm volatile("s_nop 0"); dma<!ATR>(src, aoff[n]); }
        if (has_extra(si)) { m0_set(dAx, si * A_SLOT); asm volatile("s_nop 0"); dma<!ATR>(src, aext); }
    }
    DI void issue_b(const char* src, int si, int n0) const {
#pragma unroll
        for (int n = n0; n < n0 + 2; ++n) { m0_set(dB, si * B_SLOT + n * 1024); asm volatile("s_nop 0"); dma<false>(src, boff[n]); }
    }
    DI void pro_start(const char* a0, const char* a1, const char* a2, const char* b0, const char* b1) const {
        issue_b(b0, 0, 0); issue_a(a0, 0); issue_b(b0, 0, 2); issue_a(a1, 1); issue_b(b1, 1, 0); issue_a(a2, 2);
    }
    DI void pro_finish() {
        wait_bar<2 * NBASE + 2>();
#pragma unroll
        for (int i = 0; i < NF - 2; ++i) af[i] = read_a(pa, 0, i);
        bc[0] = *(LAS bf16x8*)(pb0); bc[1] = *(LAS bf16x8*)(pb0 + 2048);
    }
    template <int SC, int SBI, int NWAIT, int NMID, bool DOB, bool DOA>
    DI void substep(f32x4 (&acc)[NF][2], const char* srcB, const char* srcA) {
        constexpr bool ODD = (SC & 1) != 0;
        constexpr int SN = (SC + 1) & 3, ST = (SC + 3) & 3;
        constexpr int B0 = ODD ? 0 : 2, BT = SBI ^ (ODD ? 0 : 1);
        constexpr int STEP = NF == 9 ? 2 : 1;
        wait_bar<NWAIT>();
        const lptr pbn = ODD ? pb0 + (SBI ^ 1) * B_SLOT : pb1 + SBI * B_SLOT;
        af[NF - 2] = read_a(pa, SC * A_SLOT, NF - 2);
        af[NF - 1] = read_a(pa, SC * A_SLOT, NF - 1);
        bf16x8 bn[2];
#pragma unroll
        for (int i = 0; i < NF; ++i) {
            const int d = (i % STEP == 0) ? i / STEP : -1;
            const bool isB = DOB && d >= 0 && d < 2, isA = DOA && d >= 2 && d < 2 + NBASE;
            if (isB) m0_set(dB, BT * B_SLOT + (B0 + d) * 1024);
            if (isA) m0_set(dA, ST * A_SLOT + (d - 2) * 4096);
            if (i == 1) bn[0] = *(LAS bf16x8*)(pbn);
            SB();
            acc[i][0] = SWAP ? MFMA16(bc[0], af[i], acc[i][0]) : MFMA16(af[i], bc[0], acc[i][0]);
            SB();
            if (isB) dma<false>(srcB, boff[B0 + d]);
            if (isA) dma<!ATR>(srcA, aoff[d - 2]);
            SB();
            acc[i][1] = SWAP ? MFMA16(bc[1], af[i], acc[i][1]) : MFMA16(af[i], bc[1], acc[i][1]);
            SB();
            if (i < NF - 2) af[i] = read_a(pa, SN * A_SLOT, i);
            if (i == NF - 2) {
                if (NMID >= 0) asm volatile("s_waitcnt vmcnt(%0)" :: "n"(NMID < 0 ? 0 : NMID) : "memory");
                bn[1] = *(LAS bf16x8*)(pbn + 2048);
            }
            SB();
        }
        if (DOA) { if (has_extra(ST)) { m0_set(dAx, ST * A_SLOT); asm volatile("s_nop 0"); dma<!ATR>(srcA, aext); } }
        bc[0] = bn[0]; bc[1] = bn[1];
    }
    template <int MODE, int PAR, int XT = 0>
    DI void kstep(f32x4 (&acc)[NF][2], const char* bE, const char* aE, const char* bO, const char* aO) {
        substep<2 * PAR, PAR, MODE == 2 ? 0 : NS + XT, -1, MODE != 2, MODE != 2>(acc, bE, aE);
        substep<2 * PAR + 1, PAR, MODE == 2 ? 0 : NS + XT, MODE == 0 ? NBASE + NS : (MODE == 1 ? NBASE : -1), MODE == 0, MODE == 0>(acc, bO, aO);
    }
};
template <int PAR0, int NF, bool ATR, bool SWAP>
DI void gemm_steady(Gemm<NF, ATR, SWAP>& G, f32x4 (&acc)[NF][2], const char*& a, size_t asub, const char*& b, int k0, int k1) {
    int k = k0;
    if (PAR0 == 1 && k < k1) { G.template kstep<0, 1>(acc, b, a, b + 128, a + asub); a += 2 * asub; b += 128; ++k; }
    for (; k + 1 < k1; k += 2) {
        G.template kstep<0, 0>(acc, b, a, b + 128, a + asub); a += 2 * asub; b += 128;
        G.template kstep<0, 1>(acc, b, a, b + 128, a + asub); a += 2 * asub; b += 128;
    }
    if (k < k1) { G.template kstep<0, 0>(acc, b, a, b + 128, a + asub); a += 2 * asub; b += 128; }
}
template <int NF> DI void zero_acc(f32x4 (&acc)[NF][2]) {
#pragma unroll
    for (int i = 0; i < NF; ++i) { acc[i][0] = f32x4{0, 0, 0, 0}; acc[i][1] = f32x4{0, 0, 0, 0}; }
}

constexpr int F_AB = 64 * 144, F_STAGE = F_AB + 8192;
struct FFrag { bf16x8 a[5], b[2]; };
DI void fread(FFrag& f, lptr pa, lptr pb) {
    f.b[0] = *(LAS bf16x8*)(pb); f.b[1] = *(LAS bf16x8*)(pb + 2048);
#pragma unroll
    for (int i = 0; i < 5; ++i) { const s16x4 lo = trread(pa + 32 * i), hi = trread(pa + 16 * 144 + 32 * i); f.a[i] = __builtin_shufflevector(lo, hi, 0, 1, 2, 3, 4, 5, 6, 7); }
}
template <int SL, int NWAIT, bool DOI>
DI void fstep(f32x4 (&acc)[5][2], FFrag& f, lptr pa, lptr pb, const char* sa, const char* sb, unsigned dA, unsigned dB, unsigned dX, const unsigned (&aoff)[3], unsigned bo0, unsigned bo1, int w) {
    wait_bar<NWAIT>();
    constexpr int SN = (SL + 1) & 3;
    FFrag n;
#pragma unroll
    for (int i = 0; i < 5; ++i) {
        if (DOI && i < 2) m0_set(dA, SL * F_STAGE + i * 4096);
        if (DOI && i >= 2 && i < 4) m0_set(dB, SL * F_STAGE + (i - 2) * 1024);
        if (i < 2) n.b[i] = *(LAS bf16x8*)(pb + SN * F_STAGE + i * 2048);
        SB();
        acc[i][0] = MFMA16(f.a[i], f.b[0], acc[i][0]);
        SB();
        if (DOI && i < 2) dma<false>(sa, aoff[i]);
        if (DOI && i >= 2 && i < 4) dma<true>(sb, i == 2 ? bo0 : bo1);
        { const s16x4 lo = trread(pa + SN * F_STAGE + 32 * i), hi = trread(pa + SN * F_STAGE + 16 * 144 + 32 * i); n.a[i] = __builtin_shufflevector(lo, hi, 0, 1, 2, 3, 4, 5, 6, 7); }
        SB();
        acc[i][1] = MFMA16(f.a[i], f.b[1], acc[i][1]);
        SB();
    }
    if (DOI && w == SL) { m0_set(dX, SL * F_STAGE); asm volatile("s_nop 0"); dma<false>(sa, aoff[2]); }
    f = n;
}
struct FinalCtx { unsigned aoff[3]; };
DI void final_aoff(unsigned (&aoff)[3], int w, int lane) {
#pragma unroll
    for (int n = 0; n < 3; ++n) {
        const int id = n < 2 ? (n * 4 + w) * 64 + lane : 512 + lane, rho = id / 9, c = id - rho * 9;
        aoff[n] = (unsigned)rho2k(rho) * (DM * 2) + (unsigned)c * 16u;
    }
}
DI void final_weights(const Params& P, unsigned lds0, int e, int k, int w, const unsigned (&aoff)[3]) {
    const char* src = (const char*)P.wout + 72 * e * 2 + (size_t)k * 64 * DM * 2;
    dma_now<false>(src, aoff[0], lds0 + k * F_STAGE + w * 1024);
    dma_now<false>(src, aoff[1], lds0 + k * F_STAGE + (4 + w) * 1024);
    if (w == k) dma_now<false>(src, aoff[2], lds0 + k * F_STAGE + 8192);
}
DI void final_tile(const Params& P, __amdgpu_buffer_rsrc_t rs, u64 tag, lptr lds, unsigned lds0, int tb, int e, int w, int lane) {
    const int d0 = 72 * e;
    const int th = w & 1, kh = w >> 1;
    int g = lane >> 4; const int q = (lane & 15) >> 2, p = lane & 3;
    char* ws = P.ws;
    unsigned aoff[3], boffo[2];
    final_aoff(aoff, w, lane);
    const unsigned bu0 = (unsigned)(w * 2048 + lane * 16), bu1 = bu0 + 1024;
    b_offsets_rowmajor<2>(boffo, 16 * w, lane, DM * 2);
    const lptr pa = lds + (32 * kh + 4 * g + q) * 144 + 8 * p;
    const lptr pb = lds + F_AB + (32 * th + (lane & 15)) * 128 + (((4 * kh + g) ^ (lane & 7)) * 16);
    const unsigned dA = lds0 + w * 1024, dB = lds0 + F_AB + w * 2048, dX = lds0 + 8192;
    f32x4 accb[5][2], acca[5][2];
    zero_acc<5>(accb); zero_acc<5>(acca);
    const char* wout = (const char*)P.wout + d0 * 2;
    const char* wo = (const char*)P.wo + d0 * 2;
    const char* ub = ws + WS_U + (size_t)tb * 36 * 8192;
    const char* obp = ws + WS_O + (size_t)tb * 64 * DM * 2;
    final_weights(P, lds0, e, 2, w, aoff);
    final_weights(P, lds0, e, 3, w, aoff);
    if (w == 0) await<4>(ws, WS_F3 + (unsigned)(((tb >> 1) * 4 + 2 * (tb & 1)) * 16 + (lane & 31)) * 8, tag);
    asm volatile("s_barrier" ::: "memory");
#pragma unroll
    for (int k = 0; k < 4; ++k) {
        dma_now<true>(ub + k * 8192, bu0, dB + k * F_STAGE);
        dma_now<true>(ub + k * 8192, bu1, dB + k * F_STAGE + 1024);
    }
    TL(9);
    FFrag f;
    wait_bar<6>();
    fread(f, pa, pb);
#define FSRC(kn) ((kn) < 36 ? wout + (size_t)(kn) * 64 * DM * 2 : wo + (size_t)((kn) - 36) * 64 * DM * 2), ((kn) < 36 ? ub + (size_t)(kn) * 8192 : obp + ((kn) - 36) * 128)
#define FB0(kn) ((kn) < 36 ? bu0 : boffo[0]), ((kn) < 36 ? bu1 : boffo[1])
#define FSTEP(SL, NW, acc_, kn) fstep<SL, NW, true>(acc_, f, pa, pb, FSRC(kn), dA, dB, dX, aoff, FB0(kn), w)
    FSTEP(0, 4, accb, 4);
    FSTEP(1, 2 + 4, accb, 5);
    FSTEP(2, 8, accb, 6);
    FSTEP(3, 8, accb, 7);
    TLC(24); TL(26);
    for (int k = 4; k < 32; k += 4) {
        fstep<0, 8, true>(accb, f, pa, pb, wout + (size_t)(k + 4) * 64 * DM * 2, ub + (size_t)(k + 4) * 8192, dA, dB, dX, aoff, bu0, bu1, w);
        fstep<1, 8, true>(accb, f, pa, pb, wout + (size_t)(k + 5) * 64 * DM * 2, ub + (size_t)(k + 5) * 8192, dA, dB, dX, aoff, bu0, bu1, w);
        fstep<2, 8, true>(accb, f, pa, pb, wout + (size_t)(k + 6) * 64 * DM * 2, ub + (size_t)(k + 6) * 8192, dA, dB, dX, aoff, bu0, bu1, w);
        fstep<3, 8, true>(accb, f, pa, pb, wout + (size_t)(k + 7) * 64 * DM * 2, ub + (size_t)(k + 7) * 8192, dA, dB, dX, aoff, bu0, bu1, w);
    }
    TLC(25); TL(27);
    if (w == 0) await<4>(ws, WS_F4 + (unsigned)(tb * 32 + (lane & 31)) * 8, tag);
    asm volatile("s_barrier" ::: "memory");
    FSTEP(0, 8, accb, 36);
    FSTEP(1, 8, accb, 37);
    FSTEP(2, 8, accb, 38);
    FSTEP(3, 8, accb, 39);
    FSTEP(0, 8, acca, 40);
    FSTEP(1, 8, acca, 41);
    FSTEP(2, 8, acca, 42);
    FSTEP(3, 8, acca, 43);
    FSTEP(0, 8, acca, 44);
    fstep<1, 8, false>(acca, f, pa, pb, nullptr, nullptr, dA, dB, dX, aoff, 0, 0, w);
    fstep<2, 4, false>(acca, f, pa, pb, nullptr, nullptr, dA, dB, dX, aoff, 0, 0, w);
    fstep<3, 0, false>(acca, f, pa, pb, nullptr, nullptr, dA, dB, dX, aoff, 0, 0, w);
    fstep<0, 0, false>(acca, f, pa, pb, nullptr, nullptr, dA, dB, dX, aoff, 0, 0, w);
    TL(10);
    lane = opq(lane); g = lane >> 4;
    float gt[2];
#pragma unroll
    for (int tt = 0; tt < 2; ++tt) gt[tt] = __uint_as_float(ld4(rs, WS_GATE + (unsigned)(tb * 64 + 32 * th + 16 * tt + (lane & 15)) * 4));
#pragma unroll
    for (int i = 0; i < 5; ++i)
#pragma unroll
        for (int tt = 0; tt < 2; ++tt) accb[i][tt] = acca[i][tt] * gt[tt] + accb[i][tt] * (1.0f - gt[tt]);
    WAIT_BAR0();
    const lptr xch = lds + th * 10240 + lane * 16;
    if (kh == 1) {
#pragma unroll
        for (int i = 0; i < 5; ++i)
#pragma unroll
            for (int tt = 0; tt < 2; ++tt) *(LAS f32x4*)(xch + (i * 2 + tt) * 1024) = accb[i][tt];
    }
    WAIT_BAR0();
    const lptr st = lds + 20480;
    if (kh == 0) {
#pragma unroll
        for (int i = 0; i < 5; ++i)
#pragma unroll
            for (int tt = 0; tt < 2; ++tt) {
                const f32x4 v = accb[i][tt] + *(LAS f32x4*)(xch + (i * 2 + tt) * 1024);
                u32x2 o; o[0] = pk2(v[0], v[1]); o[1] = pk2(v[2], v[3]);
                *(LAS u32x2*)(st + (32 * th + 16 * tt + (lane & 15)) * 160 + (16 * i + 4 * g) * 2) = o;
            }
    }
    WAIT_BAR0();
    for (int id = threadIdx.x; id < 64 * 9; id += 256) {
        const int row = id / 9, c = id - row * 9;
        *(u32x4*)((char*)P.out + (size_t)(tb * 64 + row) * (DM * 2) + d0 * 2 + c * 16) = *(LAS u32x4*)(st + row * 160 + c * 16);
    }
}

DI void role_g(const Params& P, u64 tag, lptr lds, unsigned lds0, int idx, int w, int lane0) {
    __builtin_amdgcn_s_setprio(PRIO_G1);
    const int x = idx & 7, y = idx >> 3;
    const int r = (x >> 1) * 4 + (y >> 3), j = (x & 1) * 8 + (y & 7);
    int lane = lane0;
    char* ws = P.ws;
    const __amdgpu_buffer_rsrc_t rs = ws_rsrc(ws);
    f32x4 acc[9][2];
    u32x4 z1[9];
    {
        Gemm<9, true> G; G.init(lds, lds0, w, lane);
        G.set_a(lane, 4608 * 2);
        b_offsets_rowmajor<4>(G.boff, 32 * w, lane, DM * 2);
        constexpr size_t WSUB = (size_t)32 * 4608 * 2;
        const char* wz2 = (const char*)P.win + (size_t)(FH + 144 * j) * 2;
        const char* wz1 = (const char*)P.win + (size_t)(144 * j) * 2;
        const char* xb = (const char*)P.ax + (size_t)r * 128 * DM * 2;
        G.pro_start(wz2, wz2 + WSUB, wz2 + 2 * WSUB, xb, xb + 128);
        G.pro_finish();
        TL(1);
        zero_acc<9>(acc);
        {
            const char* a = wz2 + 3 * WSUB; const char* b = xb + 128;
            gemm_steady<0>(G, acc, a, WSUB, b, 0, 7);
            G.template kstep<0, 1>(acc, xb + 8 * 128, wz2 + 17 * WSUB, xb, wz1);
            G.template kstep<0, 0>(acc, xb, wz1 + WSUB, xb + 128, wz1 + 2 * WSUB);
        }
        TL(2);
        lane = opq(lane0);
        u32x4 zp[9];
        {
            float mean[2], m2[2];
#pragma unroll
            for (int tt = 0; tt < 2; ++tt) {
                float s = 0.f;
#pragma unroll
                for (int i = 0; i < 9; ++i)
#pragma unroll
                    for (int e = 0; e < 4; ++e) { acc[i][tt][e] = gelu_tanh(acc[i][tt][e]); s += acc[i][tt][e]; }
                s = red32(red16(s));
                mean[tt] = s * (1.0f / 144.0f);
                float qq = 0.f;
#pragma unroll
                for (int i = 0; i < 9; ++i) {
#pragma unroll
                    for (int e = 0; e < 4; ++e) { const float d = acc[i][tt][e] - mean[tt]; qq = __builtin_fmaf(d, d, qq); }
                }
                m2[tt] = red32(red16(qq));
            }
            if (lane < 32) {
                const int tt = lane >> 4;
                const float mu = tt ? mean[1] : mean[0], mm = tt ? m2[1] : m2[0];
                stf(ws, WS_XBUF + (unsigned)((r * 16 + j) * 128 + 32 * w + lane) * 8, ((u64)__float_as_uint(mm) << 32) | __float_as_uint(mu));
            }
#pragma unroll
            for (int i = 0; i < 9; ++i)
                zp[i] = u32x4{pk2(acc[i][0][0], acc[i][0][1]), pk2(acc[i][0][2], acc[i][0][3]), pk2(acc[i][1][0], acc[i][1][1]), pk2(acc[i][1][2], acc[i][1][3])};
            publish(ws, WS_F1 + (unsigned)((r * 4 + w) * 16 + j) * 8, tag, lane);
        }
        TL(3);
        await<4>(ws, WS_F1 + (unsigned)((r * 4 + w) * 16 + (lane & 15)) * 8, tag);
        TL(16);
        {
            const int g = lane >> 4;
            const unsigned xo = WS_XBUF + (unsigned)((r * 16 + (lane >> 5) * 8) * 128 + 32 * w + (lane & 31)) * 8;
            float m[8], q2[8], sm = 0.f;
#pragma unroll
            for (int e = 0; e < 8; ++e) { const u64 v = ldf(ws, xo + e * 1024); m[e] = __uint_as_float((unsigned)v); q2[e] = __uint_as_float((unsigned)(v >> 32)); sm += m[e]; }
            const float mean = red32(sm) * (1.0f / 16.0f);
            float qq = 0.f;
#pragma unroll
            for (int e = 0; e < 8; ++e) { const float d = m[e] - mean; qq += q2[e] + 144.0f * d * d; }
            qq = red32(qq);
            const float rstd = 1.0f / sqrtf(qq * (1.0f / 2304.0f) + 1e-5f);
            float ra[2], rb[2];
#pragma unroll
            for (int tt = 0; tt < 2; ++tt) { ra[tt] = __shfl(rstd, 16 * tt + (lane & 15)); rb[tt] = -__shfl(mean, 16 * tt + (lane & 15)) * ra[tt]; }
            const lptr st = lds + 69632 + w * 3072;
            const lptr rd = st + (4 * g + ((lane & 15) >> 2)) * 96 + 8 * (lane & 3);
            const unsigned dst = WS_Z2N + (unsigned)(j * 64 + 4 * r + w) * 9216 + lane * 16;
#pragma unroll
            for (int p3 = 0; p3 < 3; ++p3) {
#pragma unroll
                for (int tt = 0; tt < 2; ++tt) {
                    const int gp = 2 * tt + ((lane & 15) >> 3), h = (lane >> 2) & 1, ii = lane & 3;
                    const lptr rowp = st + (16 * h + 4 * gp + ii) * 96 + 8 * g;
#pragma unroll
                    for (int c3 = 0; c3 < 3; ++c3) {
                        const int i = 3 * p3 + c3;
                        u32x2 v;
                        v[0] = pk2(__builtin_fmaf(bflo(zp[i][2 * tt]), ra[tt], rb[tt]), __builtin_fmaf(bfhi(zp[i][2 * tt]), ra[tt], rb[tt]));
                        v[1] = pk2(__builtin_fmaf(bflo(zp[i][2 * tt + 1]), ra[tt], rb[tt]), __builtin_fmaf(bfhi(zp[i][2 * tt + 1]), ra[tt], rb[tt]));
                        *(LAS u32x2*)(rowp + 32 * c3) = v;
                    }
                }
                LGKM0();
#pragma unroll
                for (int c3 = 0; c3 < 3; ++c3) {
                    const s16x4 lo = trread(rd + 32 * c3), hi = trread(rd + 16 * 96 + 32 * c3);
                    st16(rs, dst + (3 * p3 + c3) * 1024, __builtin_bit_cast(u32x4, __builtin_shufflevector(lo, hi, 0, 1, 2, 3, 4, 5, 6, 7)));
                }
                LGKM0();
            }
        }
        TL(17);
#define ZK(XT, kk) G.template kstep<0, ((kk) + 9) & 1, XT>(acc, xb + ((kk) + 1) * 128, wz1 + (2 * (kk) + 3) * WSUB, xb + ((kk) + 2) * 128, wz1 + (2 * (kk) + 4) * WSUB)
        zero_acc<9>(acc);
        ZK(9, 0);
        ZK(0, 1);
        lane = opq(lane0);
        if (lane == 0) stf(ws, WS_F2 + (unsigned)(j * 64 + 4 * r + w) * 8, tag);
        TL(18);
        ZK(1, 2); ZK(0, 3); ZK(0, 4); ZK(0, 5); ZK(0, 6);
        G.template kstep<1, 0>(acc, xb + 8 * 128, wz1 + 17 * WSUB, nullptr, nullptr);
        G.template kstep<2, 1>(acc, nullptr, nullptr, nullptr, nullptr);
        TL(4);
    }
#pragma unroll
    for (int i = 0; i < 9; ++i)
        z1[i] = u32x4{pk2(gelu_tanh(acc[i][0][0]), gelu_tanh(acc[i][0][1])), pk2(gelu_tanh(acc[i][0][2]), gelu_tanh(acc[i][0][3])),
                      pk2(gelu_tanh(acc[i][1][0]), gelu_tanh(acc[i][1][1])), pk2(gelu_tanh(acc[i][1][2]), gelu_tanh(acc[i][1][3]))};
    TL(5);
    u16 bsv[2];
#pragma unroll
    for (int tt = 0; tt < 2; ++tt) bsv[tt] = P.bsp[r * 128 + 32 * w + 16 * tt + (lane & 15)];
    {
        Gemm<9, false> G; G.init(lds, lds0, w, lane);
        G.set_a(lane, 0);
        b_offsets_rowmajor<4>(G.boff, 32 * w, lane, SEQ * 2);
        const char* zb = ws + WS_Z2N + (size_t)j * 64 * 9216;
        const char* wb = (const char*)P.wsp + (size_t)r * 128 * SEQ * 2;
        if (w == 0) await<4>(ws, WS_F2 + (unsigned)(j * 64 + lane) * 8, tag);
        WAIT_BAR0();
        TL(6);
        __builtin_amdgcn_s_setprio(0);
        for (int rep = 0; rep < EXP_REP; ++rep) {
        if (rep) { WAIT_BAR0(); TL(6); }
        G.pro_start(zb, zb + 9216, zb + 2 * 9216, wb, wb + 128);
        G.pro_finish();
        zero_acc<9>(acc);
        TLC(12);
        const char* a = zb + 3 * 9216; const char* b = wb + 128;
        gemm_steady<0>(G, acc, a, (size_t)9216, b, 0, 30);
        G.template kstep<1, 0>(acc, b, a, nullptr, nullptr);
        G.template kstep<2, 1>(acc, nullptr, nullptr, nullptr, nullptr);
        TLC(13);
        }
    }
    TL(7);
    lane = opq(lane0);
    const int tbf = 2 * r + (j >> 3);
    {
        const int g = lane >> 4;
        float bs[2];
#pragma unroll
        for (int tt = 0; tt < 2; ++tt) bs[tt] = __uint_as_float((unsigned)bsv[tt] << 16);
        WAIT_BAR0();
        {
            unsigned fa[3];
            final_aoff(fa, w, lane);
            final_weights(P, lds0, j & 7, 0, w, fa);
            final_weights(P, lds0, j & 7, 1, w, fa);
        }
        const lptr st = lds + 45056 + w * 9216;
#pragma unroll
        for (int i = 0; i < 9; ++i)
#pragma unroll
            for (int tt = 0; tt < 2; ++tt) {
                u32x2 v;
                v[0] = pk2(bflo(z1[i][2 * tt]) * (acc[i][tt][0] + bs[tt]), bfhi(z1[i][2 * tt]) * (acc[i][tt][1] + bs[tt]));
                v[1] = pk2(bflo(z1[i][2 * tt + 1]) * (acc[i][tt][2] + bs[tt]), bfhi(z1[i][2 * tt + 1]) * (acc[i][tt][3] + bs[tt]));
                *(LAS u32x2*)(st + (16 * tt + (lane & 15)) * 288 + (16 * i + 4 * g) * 2) = v;
            }
        LGKM0();
        const int tb = 2 * r + (w >> 1);
#pragma unroll
        for (int n = 0; n < 9; ++n) {
            const int id = n * 64 + lane, row = id / 18, c = id - row * 18;
            const int cg = 18 * j + c, t64 = 32 * (w & 1) + row;
            st16(rs, WS_U + (unsigned)(tb * 36 + (cg >> 3)) * 8192 + t64 * 128 + (((cg & 7) ^ (t64 & 7)) * 16), *(LAS u32x4*)(st + row * 288 + c * 16));
        }
        publish(ws, WS_F3 + (unsigned)((r * 4 + w) * 16 + j) * 8, tag, lane);
        asm volatile("s_barrier" ::: "memory");
    }
    TL(8);
    lane = opq(lane0);
    final_tile(P, rs, tag, lds, lds0, tbf, j & 7, w, lane);
}

constexpr int AT_CONST = 3 * AT_CH;
DI void attn_issue(const char* kv, int ci, unsigned ldsb, int w, int lane) {
    const char* src = kv + (size_t)ci * AT_CH;
#pragma unroll
    for (int n = 0; n < 5; ++n) dma_now<true>(src, (unsigned)((n * 4 + w) * 1024 + lane * 16), ldsb + (n * 4 + w) * 1024);
    if (w == (ci & 3)) dma_now<true>(src, (unsigned)(20480 + lane * 16), ldsb + 20480);
}
#define ZERO16 (f32x16{0, 0, 0, 0, 0, 0, 0, 0, 0, 0, 0, 0, 0, 0, 0, 0})
#define BF8(x) __builtin_bit_cast(bf16x8, x)
#define GRP(S, o_, Pv, j_) do { SB(); unsigned pa_, pb_; float t0_, t1_, t2_, t3_; \
    asm volatile("v_exp_f32 %2, %6\n\tv_exp_f32 %3, %7\n\tv_exp_f32 %4, %8\n\tv_exp_f32 %5, %9\n\tv_cvt_pk_bf16_f32 %0, %2, %3\n\tv_cvt_pk_bf16_f32 %1, %4, %5" \
        : "=&v"(pa_), "=&v"(pb_), "=&v"(t0_), "=&v"(t1_), "=&v"(t2_), "=&v"(t3_) : "v"(S[o_]), "v"(S[o_ + 1]), "v"(S[o_ + 2]), "v"(S[o_ + 3])); \
    Pv[j_] = pa_; Pv[j_ + 1] = pb_; SB(); } while (0)
DI void attn_step(f32x16 (&o)[2], f32x16& sA, u32x4& p1, bf16x8& kf1, bf16x8& kf2, bf16x8& vf1, const bf16x8 (&qf1)[2], const u32x4 (&qf2)[2],
                  lptr& pv, unsigned vstep, lptr pk1n  , lptr pk2n  ) {
    f32x16 sB; u32x4 p0;
    sB = MFMA32(kf1, qf1[1], ZERO16);
    const bf16x8 vf0 = *(LAS bf16x8*)(pv); pv += vstep;      GRP(sA, 0, p0, 0);
    o[1] = MFMA32(vf1, BF8(p1), o[1]);
    const bf16x8 nk1 = *(LAS bf16x8*)(pk1n);                 GRP(sA, 4, p0, 2);
    sB = MFMA32(kf2, BF8(qf2[1]), sB);
    const bf16x8 nv1 = *(LAS bf16x8*)(pv); pv += vstep;      GRP(sA, 8, p1, 0);
    o[0] = MFMA32(vf0, BF8(p0), o[0]);
    const bf16x8 nk2 = *(LAS bf16x8*)(pk2n);                 GRP(sA, 12, p1, 2);
    sA = MFMA32(nk1, qf1[0], ZERO16);                        GRP(sB, 0, p0, 0);
    o[0] = MFMA32(nv1, BF8(p1), o[0]);                       GRP(sB, 4, p0, 2);
    sA = MFMA32(nk2, BF8(qf2[0]), sA);                       GRP(sB, 8, p1, 0);
    o[1] = MFMA32(vf0, BF8(p0), o[1]);                       GRP(sB, 12, p1, 2);
    kf1 = nk1; kf2 = nk2; vf1 = nv1;
}
struct Prefetch {
    const char* ws; const char* zq;
    const char* wq;
    unsigned f2; u64 tag, seen;
    int k;
    unsigned lds0;
    DI void step(int ci, int w, int lane) {
        if (ci < 4 && w < 2) touch(lds0, wq + (size_t)(lane & 15) * (SEQ * 2) + (ci * 8 + w * 4 + (lane >> 4)) * 128);
        if (k < 0) {
            if (__builtin_amdgcn_readfirstlane(__all(seen == tag))) k = 0; else seen = ldf(ws, f2);
        }
        if (k >= 0 && k < 32) {
            const int kk = k + lane / 9;
            touch_sc1(lds0, zq + (size_t)(kk < 31 ? kk : 31) * 18432 + (lane % 9) * 128);
            k += (k == 0) ? 7 : 5;
        }
    }
};
template <bool PF>
DI void attn_sweep(f32x16 (&o)[2], const bf16x8 (&qf1)[2], const u32x4 (&qf2)[2], const char* kv, lptr lds, unsigned lds0, int w, int lane, Prefetch& pf) {
    const int row = lane & 31, hh = lane >> 5;
    const lptr pk1 = lds + lane * 16, pk2 = lds + AT_K2 + row * 16;
    const unsigned vmul = row < 18 ? 1u : 0u, vstep = vmul * 576u;
    const lptr vbase = row < 18 ? lds + AT_V + hh * 288 + row * 16 : lds + AT_CONST + (row == 18 ? 0 : 16);
    wait_bar<5>();
    bf16x8 kf1 = *(LAS bf16x8*)(pk1), kf2 = *(LAS bf16x8*)(pk2), vf1 = kf1;
    f32x16 sA = MFMA32(kf1, qf1[0], ZERO16);
    sA = MFMA32(kf2, BF8(qf2[0]), sA);
    asm volatile("s_nop 7\n\ts_nop 7" : "+v"(sA));
    u32x4 p1 = {0u, 0u, 0u, 0u};
#pragma unroll
    for (int b = 0; b < 2; ++b) o[b] = ZERO16;
    unsigned cur = 0;
    for (int ci = 0; ci < 8; ++ci) {
        const unsigned nxt = (cur == 2 * AT_CH) ? 0u : cur + AT_CH;
        lptr pv = vbase + vmul * cur;
#pragma unroll
        for (int kb = 0; kb < 7; ++kb) attn_step(o, sA, p1, kf1, kf2, vf1, qf1, qf2, pv, vstep, pk1 + cur + (kb + 1) * 1024, pk2 + cur + (kb + 1) * 512);
        wait_bar<0>();
        if (ci + 2 < 8) attn_issue(kv, ci + 2, lds0 + (nxt == 2 * AT_CH ? 0u : nxt + AT_CH), w, lane);
        if (PF) pf.step(ci + 1, w, lane);
        const unsigned kn = (ci + 1 < 8) ? nxt : cur;
        attn_step(o, sA, p1, kf1, kf2, vf1, qf1, qf2, pv, vstep, pk1 + kn, pk2 + kn);
        cur = nxt;
    }
    asm volatile("s_nop 1" : "+v"(p1));
    o[1] = MFMA32(vf1, BF8(p1), o[1]);
    asm volatile("" : "+v"(o[0]), "+v"(o[1]), "+v"(sA));
}
DI void attn_rowmax(float (&mx)[2], const bf16x8 (&qf1)[2], const u32x4 (&qf2)[2], const char* kv, lptr lds, unsigned lds0, int w, int lane) {
    mx[0] = -INFINITY; mx[1] = -INFINITY;
#pragma unroll 1
    for (int ci = 0; ci < 8; ++ci) {
        WAIT_BAR0();
        attn_issue(kv, ci, lds0, w, lane);
        WAIT_BAR0();
#pragma unroll 1
        for (int kb = 0; kb < 8; ++kb) {
            const bf16x8 kf1 = *(LAS bf16x8*)(lds + lane * 16 + kb * 1024), kf2 = *(LAS bf16x8*)(lds + AT_K2 + (lane & 31) * 16 + kb * 512);
#pragma unroll
            for (int b = 0; b < 2; ++b) {
                f32x16 s = MFMA32(kf1, qf1[b], ZERO16);
                s = MFMA32(kf2, BF8(qf2[b]), s);
#pragma unroll
                for (int e = 0; e < 16; ++e) mx[b] = fmaxf(mx[b], s[e]);
            }
        }
    }
    WAIT_BAR0();
}
DI void role_a(const Params& P, u64 tag, lptr lds, unsigned lds0, int idx, int w, int lane) {
    const int x = idx & 7, y = idx >> 3;
    char* ws = P.ws;
    const __amdgpu_buffer_rsrc_t rs = ws_rsrc(ws);
    const int row = lane & 31, hh = lane >> 5;
    {
        const int r = (x >> 1) * 4 + (y >> 3), c = (x & 1) * 8 + (y & 7);
        const int col0 = (108 * c) & ~7, shift = 108 * c - col0;
        constexpr size_t QSUB = (size_t)32 * 1728 * 2;
        const char* wq = (const char*)P.wqkv + (size_t)col0 * 2;
        const char* xb = (const char*)P.ax + (size_t)r * 128 * DM * 2;
        const int blk = 4 * r + w;
        f32x4 acc[7][2];
        zero_acc<7>(acc);
#define TOUCH_Z1() do { const int t = threadIdx.x, rq = (r & 3) * 144; _Pragma("unroll") for (int n = 0; n < 3; ++n) { \
            const int id = n * 256 + t, row_ = id < 576 ? id >> 2 : 0, o = (id & 3) == 3 ? 284 : (id & 3) * 128; \
            touch(lds0, (const char*)P.win + (size_t)(rq + row_) * (4608 * 2) + (144 * c) * 2 + o); } } while (0)
#define QKV_LOOP(G) do { G.init(lds, lds0, w, lane); G.set_a(lane, 1728 * 2); b_offsets_rowmajor<4>(G.boff, 32 * w, lane, DM * 2); \
            G.pro_start(wq, wq + QSUB, wq + 2 * QSUB, xb, xb + 128); G.pro_finish(); TL(1); \
            const char* a_ = wq + 3 * QSUB; const char* b_ = xb + 128; \
            gemm_steady<0>(G, acc, a_, QSUB, b_, 0, 7); \
            G.template kstep<1, 1>(acc, b_, a_, nullptr, nullptr); \
            G.template kstep<2, 0>(acc, nullptr, nullptr, nullptr, nullptr); TL(2); \
            asm volatile("s_waitcnt lgkmcnt(0)\n\ts_barrier" ::: "memory"); } while (0)
        if (c >= 11) {
            Gemm<7, true, true> G;
            QKV_LOOP(G);
            TL(27);
            const int ln = opq(lane), g = ln >> 4;
            const lptr V = lds + w * 6912;
#pragma unroll
            for (int i = 0; i < 7; ++i) {
                const int n = 16 * i + (ln & 15) - shift, uu = n / 18, d = n - 18 * uu;
                if (n >= 0 && n < 108) {
#pragma unroll
                    for (int tt = 0; tt < 2; ++tt)
                        *(LAS u32x2*)(V + uu * 1152 + tt * 576 + (g & 1) * 288 + d * 16 + (g >> 1) * 8) = u32x2{pk2(acc[i][tt][0], acc[i][tt][1]), pk2(acc[i][tt][2], acc[i][tt][3])};
                }
            }
            LGKM0();
            TL(25); TL(26);
#pragma unroll
            for (int n = 0; n < 7; ++n) {
                const int id = n * 64 + ln, uu = id / 72, rest = id - uu * 72;
                if (id < 432) st16(rs, WS_KV + (unsigned)(((6 * c + uu) & 31) * 8 + (blk >> 3)) * AT_CH + AT_V + (blk & 7) * 1152 + rest * 16, *(LAS u32x4*)(V + id * 16));
            }
        } else {
            Gemm<7, true> G;
            QKV_LOOP(G);
            TL(27);
            const int ln = opq(lane), g = ln >> 4, row = ln & 31, hh = ln >> 5;
            constexpr int TP = 232;
            constexpr float QS = 0.23570226039551584f * 1.4426950408889634f;
            const lptr T = lds + w * (32 * TP);
#pragma unroll
            for (int i = 0; i < 7; ++i) {
                float sc[4];
#pragma unroll
                for (int e = 0; e < 4; ++e) sc[e] = (col0 + 16 * i + 4 * g + e < 576) ? QS : 1.0f;
#pragma unroll
                for (int tt = 0; tt < 2; ++tt) {
                    u32x2 v; v[0] = pk2(acc[i][tt][0] * sc[0], acc[i][tt][1] * sc[1]); v[1] = pk2(acc[i][tt][2] * sc[2], acc[i][tt][3] * sc[3]);
                    *(LAS u32x2*)(T + (16 * tt + (ln & 15)) * TP + (16 * i + 4 * g) * 2) = v;
                }
            }
            LGKM0();
            TL(25);
            u32x4 fv[6]; unsigned fe[6];
#pragma unroll
            for (int uu = 0; uu < 6; ++uu) {
                const int n0 = shift + 18 * uu;
                const lptr src = T + row * TP + (n0 + 8 * hh) * 2;
#pragma unroll
                for (int e = 0; e < 4; ++e) fv[uu][e] = *(LAS unsigned*)(src + 4 * e);
                fe[uu] = *(LAS unsigned*)(T + row * TP + (n0 + 16) * 2);
            }
            asm volatile("" :: "v"(fv[5]), "v"(fe[5]));
            TL(26);
#pragma unroll
            for (int uu = 0; uu < 6; ++uu) {
                const int u = 6 * c + uu, type = u >> 5, h = u & 31, n0 = shift + 18 * uu;
                const unsigned kvb = WS_KV + (unsigned)(h * 8 + (blk >> 3)) * AT_CH;
                const u32x4 v = fv[uu]; const unsigned ex = fe[uu];
                if (type == 0) {
                    st16(rs, WS_QF1 + (unsigned)(h * 64 + blk) * 1024 + ln * 16, v);
                    if (hh == 0) st4(rs, WS_Q2 + (unsigned)(h * SEQ + blk * 32 + row) * 4, ex);
                } else if (type == 1) {
                    st16(rs, kvb + (blk & 7) * 1024 + ln * 16, v);
                    if (hh == 0) st16(rs, kvb + AT_K2 + ((blk & 7) * 32 + row) * 16, u32x4{ex, 0x3f803f80u, 0x00003f80u, 0u});
                    float s = 0.f;
#pragma unroll
                    for (int e = 0; e < 4; ++e) s += bflo(v[e]) * bflo(v[e]) + bfhi(v[e]) * bfhi(v[e]);
                    if (hh == 0) s += bflo(ex) * bflo(ex) + bfhi(ex) * bfhi(ex);
                    s = red32(s);
                    s = fmaxf(s, __builtin_bit_cast(float, __builtin_amdgcn_update_dpp(0, __builtin_bit_cast(int, s), 0xB1, 0xF, 0xF, true)));
                    s = fmaxf(s, __builtin_bit_cast(float, __builtin_amdgcn_update_dpp(0, __builtin_bit_cast(int, s), 0x4E, 0xF, 0xF, true)));
                    s = fmaxf(s, __builtin_bit_cast(float, __builtin_amdgcn_update_dpp(0, __builtin_bit_cast(int, s), 0x141, 0xF, 0xF, true)));
                    s = fmaxf(s, __builtin_bit_cast(float, __builtin_amdgcn_update_dpp(0, __builtin_bit_cast(int, s), 0x140, 0xF, 0xF, true)));
                    { auto rr = __builtin_amdgcn_permlane16_swap(__float_as_uint(s), __float_as_uint(s), false, false); s = fmaxf(__uint_as_float(rr[0]), __uint_as_float(rr[1])); }
                    if (ln == 0) st4(rs, WS_KMAX + (unsigned)(h * 64 + blk) * 4, __float_as_uint(s));
                } else if (row < 18) {
#pragma unroll
                    for (int s = 0; s < 2; ++s) {
                        u32x4 vv;
#pragma unroll
                        for (int e = 0; e < 4; ++e) {
                            const int k0 = 16 * s + 8 * (e >> 1) + 4 * hh + 2 * (e & 1);
                            const unsigned lo = *(LAS u16*)(T + k0 * TP + (n0 + row) * 2), hi = *(LAS u16*)(T + (k0 + 1) * TP + (n0 + row) * 2);
                            vv[e] = lo | (hi << 16);
                        }
                        st16(rs, kvb + AT_V + (blk & 7) * 1152 + s * 576 + hh * 288 + row * 16, vv);
                    }
                }
            }
        }
        TL(9);
        publish(ws, WS_F5 + (unsigned)(c * 64 + blk) * 8, tag, lane);
        TL(3);
        TOUCH_Z1();
        {
            const int t = threadIdx.x;
            touch(lds0, (const char*)P.wsp + (size_t)(idx * 256 + t) * 128);
            const int id = idx * 102 + (t < 102 ? t : 0);
            touch(lds0, id < 20736 ? (const char*)P.wout + (size_t)id * 128 : (const char*)P.wo + (size_t)(id - 20736 < 5184 ? id - 20736 : 0) * 128);
        }
    }
    const int h = x * 4 + (y >> 3), qc = y & 7;
    const char* kv = ws + WS_KV + (size_t)h * 8 * AT_CH;
    {
        const int grow = qc * 256 + w * 64 + 2 * h + hh;
        float s = 0.f;
#pragma unroll
        for (int n = 0; n < 3; ++n) {
            const int c = row + 32 * n;
            if (c < 72) {
                const u32x4 xv = *(const u32x4*)(P.ax + (size_t)grow * DM + c * 8), gv = *(const u32x4*)(P.gw + c * 8);
#pragma unroll
                for (int e = 0; e < 4; ++e) s += bflo(xv[e]) * bflo(gv[e]) + bfhi(xv[e]) * bfhi(gv[e]);
            }
        }
#pragma unroll
        for (int o = 16; o >= 1; o >>= 1) s += __shfl_xor(s, o);
        if (row == 0) st4(rs, WS_GATE + grow * 4, __float_as_uint(__builtin_amdgcn_rcpf(1.0f + __builtin_amdgcn_exp2f(-1.4426950408889634f * s))));
    }
    if (w == 0) {
        const unsigned fq = WS_F5 + (unsigned)((h / 6) * 64 + lane) * 8, fk = WS_F5 + (unsigned)(((32 + h) / 6) * 64 + lane) * 8, fv = WS_F5 + (unsigned)(((64 + h) / 6) * 64 + lane) * 8;
        for (unsigned spins = 0; spins < (1u << 16); ++spins) {
            const u64 a = ldf(ws, fq), b = ldf(ws, fk), c = ldf(ws, fv);
            if (__all(a == tag && b == tag && c == tag)) break;
            __builtin_amdgcn_s_sleep(6);
        }
    }
    if (threadIdx.x < 2) { const unsigned v = threadIdx.x == 0 ? 0x3f803f80u : 0u; *(LAS u32x4*)(lds + AT_CONST + threadIdx.x * 16) = u32x4{v, v, v, v}; }
    asm volatile("s_waitcnt lgkmcnt(0)\n\ts_barrier" ::: "memory");
    TL(4);
    const unsigned kmraw = ld4(rs, WS_KMAX + (unsigned)(h * 64 + lane) * 4);
    u32x4 qv[2]; unsigned qe[2];
#pragma unroll
    for (int b = 0; b < 2; ++b) {
        const int qb = qc * 8 + w * 2 + b;
        qv[b] = ld16(rs, WS_QF1 + (unsigned)(h * 64 + qb) * 1024 + lane * 16);
        qe[b] = ld4(rs, WS_Q2 + (unsigned)(h * SEQ + qb * 32 + row) * 4);
    }
    attn_issue(kv, 0, lds0, w, lane);
    attn_issue(kv, 1, lds0 + AT_CH, w, lane);
    TL(10);
    float kmax2 = __uint_as_float(kmraw);
#pragma unroll
    for (int o = 32; o >= 1; o >>= 1) kmax2 = fmaxf(kmax2, __shfl_xor(kmax2, o));
    bf16x8 qf1[2]; u32x4 qf2[2];
#pragma unroll
    for (int b = 0; b < 2; ++b) {
        const u32x4 v = qv[b];
        const unsigned ex = qe[b];
        float s = 0.f;
#pragma unroll
        for (int e = 0; e < 4; ++e) s += bflo(v[e]) * bflo(v[e]) + bfhi(v[e]) * bfhi(v[e]);
        if (hh == 0) s += bflo(ex) * bflo(ex) + bfhi(ex) * bfhi(ex);
        s = red32(s);
        const float M = sqrtf(s * kmax2) * 1.0078125f;
        const unsigned mb = ((__float_as_uint(M) + 0xffffu) >> 16) | 0x8000u;
        qf1[b] = __builtin_bit_cast(bf16x8, v);
        qf2[b] = hh == 0 ? u32x4{ex, mb, 0u, 0u} : u32x4{0u, 0u, 0u, 0u};
    }
    f32x16 o[2];
    TL(5); TLC(12);
    Prefetch pf;
    {
        const int r = (x >> 1) * 4 + (y >> 3), j = (x & 1) * 8 + (y & 7);
        pf.ws = ws; pf.zq = ws + WS_Z2N + (size_t)j * 64 * 9216 + ((r & 3) * 36 + w * 9) * 128;
        pf.wq = (const char*)P.wsp + (size_t)(r * 128 + (j & 7) * 16) * (SEQ * 2);
        pf.f2 = WS_F2 + (unsigned)(j * 64 + lane) * 8; pf.tag = tag; pf.seen = tag + 1; pf.lds0 = lds0;
        pf.k = -1;
        pf.step(0, w, lane);
    }
    attn_sweep<true>(o, qf1, qf2, kv, lds, lds0, w, lane, pf);
    TL(6); TLC(13);
    {
        bool bad = false;
#pragma unroll
        for (int b = 0; b < 2; ++b) { const float l = __shfl(o[b][10], row); bad |= !(l >= 8.271806125530277e-25f); }
        LAS unsigned* vote = (LAS unsigned*)(lds + AT_CONST + 32);
        if (threadIdx.x == 0) *vote = 0u;
        WAIT_BAR0();
        if (__any(bad) && lane == 0) *vote = 1u;
        WAIT_BAR0();
        if (__builtin_amdgcn_readfirstlane(*vote) != 0u) {
#pragma unroll
            for (int b = 0; b < 2; ++b) qf2[b][1] = 0u;
            float mx[2];
            attn_rowmax(mx, qf1, qf2, kv, lds, lds0, w, lane);
#pragma unroll
            for (int b = 0; b < 2; ++b) {
                const float nm = -max32(mx[b]);
                const unsigned h1 = __float_as_uint(nm) & 0xffff0000u; const float r1 = nm - __uint_as_float(h1);
                const unsigned h2 = __float_as_uint(r1) & 0xffff0000u; const float r2 = r1 - __uint_as_float(h2);
                const unsigned h3 = __float_as_uint(r2) & 0xffff0000u;
                if (hh == 0) { qf2[b][1] = (h1 >> 16) | h2; qf2[b][2] = h3 >> 16; }
            }
            attn_issue(kv, 0, lds0, w, lane);
            attn_issue(kv, 1, lds0 + AT_CH, w, lane);
            attn_sweep<false>(o, qf1, qf2, kv, lds, lds0, w, lane, pf);
            WAIT_BAR0();
        }
    }
    const lptr st = lds + w * 2560;
#pragma unroll
    for (int b = 0; b < 2; ++b) {
        const float inv = 1.0f / __shfl(o[b][10], row);
        const lptr rp = st + (32 * b + row) * 36 + hh * 8;
        *(LAS unsigned*)(rp + 0) = pk2(o[b][0] * inv, o[b][1] * inv);
        *(LAS unsigned*)(rp + 4) = pk2(o[b][2] * inv, o[b][3] * inv);
        *(LAS unsigned*)(rp + 16) = pk2(o[b][4] * inv, o[b][5] * inv);
        *(LAS unsigned*)(rp + 20) = pk2(o[b][6] * inv, o[b][7] * inv);
        if (hh == 0) *(LAS unsigned*)(rp + 32) = pk2(o[b][8] * inv, o[b][9] * inv);
    }
    LGKM0();
#pragma unroll
    for (int n = 0; n < 9; ++n) {
        const int id = n * 64 + lane, rr = id / 9, cc = id - rr * 9;
        st4(rs, WS_O + (unsigned)(qc * 256 + w * 64 + rr) * (DM * 2) + h * 36 + cc * 4, *(LAS unsigned*)(st + id * 4));
    }
    publish(ws, WS_F4 + (unsigned)((qc * 4 + w) * 32 + h) * 8, tag, lane);
    TL(7);
    if (idx == 0 && w == 0) {
#pragma unroll 1
        for (int n = 0; n < 4; ++n) {
            await(ws, WS_F1 + (unsigned)(((4 * n + (lane >> 4)) * 4) * 16 + (lane & 15)) * 8, tag);
            await(ws, WS_F5 + (unsigned)((4 * n + (lane >> 4)) * 64 + 4 * (lane & 15)) * 8, tag);
        }
        if (lane == 0) stf(ws, WS_NONCE, tag);
    }
    VM0();
#pragma unroll 1
    for (int n = 0; n < 64 && pf.k < 32; ++n) {
        pf.step(8, w, lane);
        VM0();
        __builtin_amdgcn_s_sleep(32);
    }
    {
        const int t = threadIdx.x;
        const int r = (x >> 1) * 4 + (y >> 3), j = (x & 1) * 8 + (y & 7), tb = 2 * r + (j >> 3);
#define WTOUCH(id_) do { const int id = (id_) < 810 ? (id_) : 0, ks = id / 18, ln = 18 * y + id % 18; \
            touch(lds0, ks < 36 ? (const char*)P.wout + (size_t)(576 * ks + ln) * 128 : (const char*)P.wo + (size_t)(576 * (ks - 36) + ln) * 128); } while (0)
        await<16>(ws, WS_F3 + (unsigned)((r * 4 + 2 * (tb & 1)) * 16 + (lane & 31)) * 8, tag);
        WTOUCH(t);
        const char* ub = ws + WS_U + (size_t)tb * 36 * 8192 + (size_t)(j & 7) * 8 * 128;
        touch_sc1(lds0, ub + (size_t)(t >> 3) * 8192 + (t & 7) * 128);
        touch_sc1(lds0, ub + (size_t)(32 + ((t >> 3) & 3)) * 8192 + (t & 7) * 128);
        WTOUCH(256 + t);
        VM0();
        __builtin_amdgcn_s_sleep(48);
        WTOUCH(512 + t); WTOUCH(768 + t);
        await<16>(ws, WS_F4 + (unsigned)(tb * 32 + (lane & 31)) * 8, tag);
        { const int id = (t < 72 ? t : 0) + (j & 7) * 72; touch_sc1(lds0, ws + WS_O + (size_t)(tb * 64 + id / 9) * (DM * 2) + (id % 9) * 128); }
        VM0();
    }
    TL(8);
}

__global__ __launch_bounds__(256, 2) void gated_mix_kernel(Params P) {
    extern __shared__ __attribute__((aligned(16))) char smem[];
    const lptr lds = (lptr)smem;
    const unsigned lds0 = (unsigned)(uintptr_t)lds;
    const int w = __builtin_amdgcn_readfirstlane(threadIdx.x >> 6), lane = threadIdx.x & 63;
    TL(0); TLC(14);
    const u64 tag = ldf(P.ws, WS_NONCE) + 1ull;
    {
        const int t = threadIdx.x, id = blockIdx.x * 148 + (t < 148 ? t : 0);
        const char* p = id < 18432 ? (const char*)P.ax + (size_t)id * 128
                      : id < 18432 + 41472 ? (const char*)P.win + (size_t)(id - 18432) * 128
                      : (const char*)P.wqkv + (size_t)(id - 59904 < 15552 ? id - 59904 : 0) * 128;
        if (w < 3) touch(lds0, p);
        else {
            const char* code = (const char*)__builtin_amdgcn_s_getpc();
            const char* base = (const char*)((uintptr_t)code & ~(uintptr_t)127);
            const int ln = (blockIdx.x >> 3) * 20 + (lane < 20 ? lane : 0);
            if (ln * 128 < CODE_BYTES) touch(lds0, base + ln * 128);
        }
    }
    if (blockIdx.x < 256) role_g(P, tag, lds, lds0, blockIdx.x, w, lane);
    else role_a(P, tag, lds, lds0, blockIdx.x - 256, w, lane);
    TL(11); TLC(15);
}

extern "C" void kernel_launch(void* const* d_in, const int* in_sizes, int n_in, void* d_out, int out_size, void* d_ws, size_t ws_size, hipStream_t stream) {
    Params P;
    P.ax = (const u16*)d_in[0]; P.wqkv = (const u16*)d_in[1]; P.wo = (const u16*)d_in[2]; P.win = (const u16*)d_in[4];
    P.wsp = (const u16*)d_in[5]; P.bsp = (const u16*)d_in[6]; P.wout = (const u16*)d_in[7]; P.gw = (const u16*)d_in[8];
    P.out = (u16*)d_out; P.ws = (char*)d_ws;
    (void)hipFuncSetAttribute((const void*)gated_mix_kernel, hipFuncAttributeMaxDynamicSharedMemorySize, LDS_BYTES);
    gated_mix_kernel<<<512, 256, LDS_BYTES, stream>>>(P);
}
```

```cpp
#include <hip/hip_runtime.h>
#include <stdint.h>

typedef unsigned short u16;
typedef unsigned long long u64;
typedef short bf16x8 __attribute__((ext_vector_type(8)));
typedef short s16x4 __attribute__((ext_vector_type(4)));
typedef float f32x2 __attribute__((ext_vector_type(2)));
typedef float f32x4 __attribute__((ext_vector_type(4)));
typedef float f32x16 __attribute__((ext_vector_type(16)));
typedef unsigned u32x2 __attribute__((ext_vector_type(2)));
typedef unsigned u32x4 __attribute__((ext_vector_type(4)));
typedef __bf16 bf16x2_t __attribute__((ext_vector_type(2)));
#define LAS __attribute__((address_space(3)))
#define GAS __attribute__((address_space(1)))
typedef LAS char* lptr;
typedef GAS u64* gu64;
#define DI __device__ __forceinline__

constexpr int SEQ = 2048, DM = 576, FH = 2304;

constexpr unsigned WS_Z2N = 0;
constexpr unsigned WS_U = WS_Z2N + 9437184;
constexpr unsigned WS_QF1 = WS_U + 9437184;
constexpr unsigned WS_Q2 = WS_QF1 + 2097152;
constexpr unsigned AT_CH = 21504, AT_K2 = 8192, AT_V = 12288;
constexpr unsigned WS_KV = WS_Q2 + 262144;
constexpr unsigned WS_KMAX = WS_KV + 32 * 8 * AT_CH;
constexpr unsigned WS_O = WS_KMAX + 8192;
constexpr unsigned WS_GATE = WS_O + 2359296;
constexpr unsigned WS_XBUF = WS_GATE + 8192;
constexpr unsigned WS_F1 = WS_XBUF + 262144;
constexpr unsigned WS_F2 = WS_F1 + 8192;
constexpr unsigned WS_F3 = WS_F2 + 8192;
constexpr unsigned WS_F4 = WS_F3 + 8192;
constexpr unsigned WS_F5 = WS_F4 + 8192;
constexpr unsigned WS_NONCE = WS_F5 + 8192;
constexpr unsigned WS_END = WS_NONCE + 256;

struct Params {
    const u16* ax; const u16* wqkv; const u16* wo; const u16* win; const u16* wsp;
    const u16* bsp; const u16* wout; const u16* gw; u16* out; char* ws;
};

DI unsigned pk2(float lo, float hi) { f32x2 v = {lo, hi}; return __builtin_bit_cast(unsigned, __builtin_convertvector(v, bf16x2_t)); }
DI float bflo(unsigned u) { return __uint_as_float(u << 16); }
DI float bfhi(unsigned u) { return __uint_as_float(u & 0xffff0000u); }
DI float red16(float x) { auto r = __builtin_amdgcn_permlane16_swap(__float_as_uint(x), __float_as_uint(x), false, false); return __uint_as_float(r[0]) + __uint_as_float(r[1]); }
DI float red32(float x) { auto r = __builtin_amdgcn_permlane32_swap(__float_as_uint(x), __float_as_uint(x), false, false); return __uint_as_float(r[0]) + __uint_as_float(r[1]); }
DI float max32(float x) { auto r = __builtin_amdgcn_permlane32_swap(__float_as_uint(x), __float_as_uint(x), false, false); return fmaxf(__uint_as_float(r[0]), __uint_as_float(r[1])); }
#define SB() __builtin_amdgcn_sched_barrier(0)
DI f32x2 gelu2(f32x2 x) {
    constexpr float C0 = -2.0f * 0.7978845608028654f * 1.4426950408889634f, C1 = C0 * 0.044715f;
    const f32x2 t = x * x, p = __builtin_elementwise_fma(t, f32x2{C1, C1}, f32x2{C0, C0}), a = x * p;
    const f32x2 d = f32x2{__builtin_amdgcn_exp2f(a.x), __builtin_amdgcn_exp2f(a.y)} + f32x2{1.0f, 1.0f};
    return x * f32x2{__builtin_amdgcn_rcpf(d.x), __builtin_amdgcn_rcpf(d.y)};
}
DI unsigned pk2v(f32x2 v) { return __builtin_bit_cast(unsigned, __builtin_convertvector(v, bf16x2_t)); }
DI int opq(int x) { asm volatile("" : "+v"(x)); return x; }
#define WAIT_BAR0() asm volatile("s_waitcnt vmcnt(0) lgkmcnt(0)\n\ts_barrier" ::: "memory")
#define LGKM0() asm volatile("s_waitcnt lgkmcnt(0)" ::: "memory")
#define VM0() asm volatile("s_waitcnt vmcnt(0)" ::: "memory")
template <int N> DI void wait_bar() {
    asm volatile("" ::: "memory");
    __builtin_amdgcn_s_waitcnt((N & 15) | ((N >> 4) << 14) | (7 << 4));
    __builtin_amdgcn_s_barrier();
    asm volatile("" ::: "memory");
}
DI s16x4 trread(lptr p) { return __builtin_amdgcn_ds_read_tr16_b64_v4i16((LAS s16x4*)p); }
#define MFMA16(a, b, c) __builtin_amdgcn_mfma_f32_16x16x32_bf16((a), (b), (c), 0, 0, 0)
#define MFMA32(a, b, c) __builtin_amdgcn_mfma_f32_32x32x16_bf16((a), (b), (c), 0, 0, 0)
DI void m0_set(unsigned base, int imm) { asm volatile("s_add_u32 m0, %0, %1" :: "s"(base), "i"(imm) : "scc", "memory"); }
template <bool SC1> DI void dma(const void* sbase, unsigned voff) {
    if constexpr (SC1) asm volatile("global_load_lds_dwordx4 %0, %1 sc1" :: "v"(voff), "s"(sbase) : "memory");
    else asm volatile("global_load_lds_dwordx4 %0, %1" :: "v"(voff), "s"(sbase) : "memory");
}
template <bool SC1> DI void dma_now(const void* sbase, unsigned voff, unsigned lds_addr) { m0_set(lds_addr, 0); asm volatile("s_nop 0"); dma<SC1>(sbase, voff); }

DI __amdgpu_buffer_rsrc_t ws_rsrc(char* ws) { return __builtin_amdgcn_make_buffer_rsrc(ws, 0, WS_END, 0x00020000); }
DI void st16(__amdgpu_buffer_rsrc_t rs, unsigned off, u32x4 v) { __builtin_amdgcn_raw_buffer_store_b128(v, rs, off, 0, 16); }
DI void st4(__amdgpu_buffer_rsrc_t rs, unsigned off, unsigned v) { __builtin_amdgcn_raw_buffer_store_b32(v, rs, off, 0, 16); }
DI u32x4 ld16(__amdgpu_buffer_rsrc_t rs, unsigned off) { return __builtin_amdgcn_raw_buffer_load_b128(rs, off, 0, 16); }
DI unsigned ld4(__amdgpu_buffer_rsrc_t rs, unsigned off) { return __builtin_amdgcn_raw_buffer_load_b32(rs, off, 0, 16); }
DI u64 ldf(const char* ws, unsigned off) { return __hip_atomic_load((gu64)(ws + off), __ATOMIC_RELAXED, __HIP_MEMORY_SCOPE_AGENT); }
DI void stf(char* ws, unsigned off, u64 v) { __hip_atomic_store((gu64)(ws + off), v, __ATOMIC_RELAXED, __HIP_MEMORY_SCOPE_AGENT); }
constexpr int LDS_DUMP = 81664;
DI void touch(unsigned lds0, const char* p) { m0_set(lds0, LDS_DUMP); asm volatile("s_nop 0\n\tglobal_load_lds_dword %0, off" :: "v"(p) : "memory"); }
DI void touch_sc1(unsigned lds0, const char* p) { m0_set(lds0, LDS_DUMP); asm volatile("s_nop 0\n\tglobal_load_lds_dword %0, off sc1" :: "v"(p) : "memory"); }
DI void publish(char* ws, unsigned flag_off, u64 tag, int lane) { VM0(); if (lane == 0) stf(ws, flag_off, tag); }
template <int SLEEP = 8> DI void await(const char* ws, unsigned flag_off, u64 tag) {
    for (unsigned spins = 0; spins < (1u << 16); ++spins) {
        if (__all(ldf(ws, flag_off) == tag)) break;
        __builtin_amdgcn_s_sleep(SLEEP);
    }
    __builtin_amdgcn_fence(__ATOMIC_ACQUIRE, "wavefront");
}

DI int rho2k(int rho) { const int r = rho & 31; return (rho & 32) + 8 * ((r >> 2) & 3) + 4 * (r >> 4) + (r & 3); }

constexpr int LDS_BYTES = 81920;
constexpr int A_SLOT = 9216, B_RING = 4 * A_SLOT, B_SLOT = 16384;
template <int NI> DI void b_offsets_rowmajor(unsigned (&off)[NI], int rw, int lane, unsigned ldb) {
#pragma unroll
    for (int n = 0; n < NI; ++n) { const int row = rw + 8 * n + (lane >> 3); off[n] = (unsigned)row * ldb + (unsigned)(((lane & 7) ^ (row & 7)) * 16); }
}
template <int NF, bool ATR, bool SWAP = false> struct Gemm {
    static constexpr int P = NF * 32, NCH = NF * 2;
    static constexpr int NBASE = (NF * 2 * 32 / 64) / 4;
    static constexpr int NS = NBASE + 2;
    unsigned aoff[NBASE], aext, boff[4];
    lptr pa, pb0, pb1;
    unsigned dA, dAx, dB;
    int w;
    bf16x8 af[NF], bc[2];
    DI void init(lptr lds, unsigned lds0, int w_, int lane) {
        const int g = lane >> 4, q = (lane & 15) >> 2, p = lane & 3;
        w = w_;
        pa = ATR ? lds + (4 * g + q) * P + 8 * p : lds + lane * 16;
        pb0 = lds + B_RING + (32 * w + (lane & 15)) * 128 + ((g ^ (lane & 7)) * 16);
        pb1 = lds + B_RING + (32 * w + (lane & 15)) * 128 + (((4 + g) ^ (lane & 7)) * 16);
        dA = lds0 + w * 1024; dB = lds0 + B_RING + w * 4096;
        dAx = lds0 + NBASE * 4096 + (NF == 9 ? 0 : w * 1024);
    }
    DI void set_a(int lane, unsigned ldb) {
#pragma unroll
        for (int n = 0; n <= NBASE; ++n) {
            const int id = n < NBASE ? (n * 4 + w) * 64 + lane : NBASE * 256 + (NF == 9 ? 0 : (w < 3 ? w : 0) * 64) + lane;
            unsigned o;
            if constexpr (ATR) { const int rho = id / NCH, c = id - rho * NCH; o = (unsigned)rho2k(rho) * ldb + (unsigned)c * 16u; } else o = (unsigned)id * 16u;
            if (n < NBASE) aoff[n] = o; else aext = o;
        }
    }
    DI bool has_extra(int slot_idx) const { return NF == 9 ? w == slot_idx : w < 3; }
    DI bf16x8 read_a(lptr p, int off, int i) const {
        if constexpr (ATR) { const s16x4 lo = trread(p + off + 32 * i), hi = trread(p + off + 16 * P + 32 * i); return __builtin_shufflevector(lo, hi, 0, 1, 2, 3, 4, 5, 6, 7); }
        else return *(LAS bf16x8*)(p + off + 1024 * i);
    }
    DI void issue_a(const char* src, int si) const {
#pragma unroll
        for (int n = 0; n < NBASE; ++n) { m0_set(dA, si * A_SLOT + n * 4096); asm volatile("s_nop 0"); dma<!ATR>(src, aoff[n]); }
        if (has_extra(si)) { m0_set(dAx, si * A_SLOT); asm volatile("s_nop 0"); dma<!ATR>(src, aext); }
    }
    DI void issue_b(const char* src, int si, int n0) const {
#pragma unroll
        for (int n = n0; n < n0 + 2; ++n) { m0_set(dB, si * B_SLOT + n * 1024); asm volatile("s_nop 0"); dma<false>(src, boff[n]); }
    }
    DI void pro_start(const char* a0, const char* a1, const char* a2, const char* b0, const char* b1) const {
        issue_b(b0, 0, 0); issue_a(a0, 0); issue_b(b0, 0, 2); issue_a(a1, 1); issue_b(b1, 1, 0); issue_a(a2, 2);
    }
    DI void pro_finish() {
        wait_bar<2 * NBASE + 2>();
#pragma unroll
        for (int i = 0; i < NF - 2; ++i) af[i] = read_a(pa, 0, i);
        bc[0] = *(LAS bf16x8*)(pb0); bc[1] = *(LAS bf16x8*)(pb0 + 2048);
    }
    template <int SC, int SBI, int NWAIT, int NMID, bool DOB, bool DOA>
    DI void substep(f32x4 (&acc)[NF][2], const char* srcB, const char* srcA) {
        constexpr bool ODD = (SC & 1) != 0;
        constexpr int SN = (SC + 1) & 3, ST = (SC + 3) & 3;
        constexpr int B0 = ODD ? 0 : 2, BT = SBI ^ (ODD ? 0 : 1);
        constexpr int STEP = NF == 9 ? 2 : 1;
        wait_bar<NWAIT>();
        const lptr pbn = ODD ? pb0 + (SBI ^ 1) * B_SLOT : pb1 + SBI * B_SLOT;
        af[NF - 2] = read_a(pa, SC * A_SLOT, NF - 2);
        af[NF - 1] = read_a(pa, SC * A_SLOT, NF - 1);
        bf16x8 bn[2];
#pragma unroll
        for (int i = 0; i < NF; ++i) {
            const int d = (i % STEP == 0) ? i / STEP : -1;
            const bool isB = DOB && d >= 0 && d < 2, isA = DOA && d >= 2 && d < 2 + NBASE;
            if (isB) m0_set(dB, BT * B_SLOT + (B0 + d) * 1024);
            if (isA) m0_set(dA, ST * A_SLOT + (d - 2) * 4096);
            if (i == 1) bn[0] = *(LAS bf16x8*)(pbn);
            SB();
            acc[i][0] = SWAP ? MFMA16(bc[0], af[i], acc[i][0]) : MFMA16(af[i], bc[0], acc[i][0]);
            SB();
            if (isB) dma<false>(srcB, boff[B0 + d]);
            if (isA) dma<!ATR>(srcA, aoff[d - 2]);
            SB();
            acc[i][1] = SWAP ? MFMA16(bc[1], af[i], acc[i][1]) : MFMA16(af[i], bc[1], acc[i][1]);
            SB();
            if (i < NF - 2) af[i] = read_a(pa, SN * A_SLOT, i);
            if (i == NF - 2) {
                if (NMID >= 0) asm volatile("s_waitcnt vmcnt(%0)" :: "n"(NMID < 0 ? 0 : NMID) : "memory");
                bn[1] = *(LAS bf16x8*)(pbn + 2048);
            }
            SB();
        }
        if (DOA) { if (has_extra(ST)) { m0_set(dAx, ST * A_SLOT); asm volatile("s_nop 0"); dma<!ATR>(srcA, aext); } }
        bc[0] = bn[0]; bc[1] = bn[1];
    }
    template <int MODE, int PAR, int XT = 0>
    DI void kstep(f32x4 (&acc)[NF][2], const char* bE, const char* aE, const char* bO, const char* aO) {
        substep<2 * PAR, PAR, MODE == 2 ? 0 : NS + XT, -1, MODE != 2, MODE != 2>(acc, bE, aE);
        substep<2 * PAR + 1, PAR, MODE == 2 ? 0 : NS + XT, MODE == 0 ? NBASE + NS : (MODE == 1 ? NBASE : -1), MODE == 0, MODE == 0>(acc, bO, aO);
    }
};
template <int PAR0, int NF, bool ATR, bool SWAP>
DI void gemm_steady(Gemm<NF, ATR, SWAP>& G, f32x4 (&acc)[NF][2], const char*& a, size_t asub, const char*& b, int k0, int k1) {
    int k = k0;
    if (PAR0 == 1 && k < k1) { G.template kstep<0, 1>(acc, b, a, b + 128, a + asub); a += 2 * asub; b += 128; ++k; }
    for (; k + 1 < k1; k += 2) {
        G.template kstep<0, 0>(acc, b, a, b + 128, a + asub); a += 2 * asub; b += 128;
        G.template kstep<0, 1>(acc, b, a, b + 128, a + asub); a += 2 * asub; b += 128;
    }
    if (k < k1) { G.template kstep<0, 0>(acc, b, a, b + 128, a + asub); a += 2 * asub; b += 128; }
}
template <int NF> DI void zero_acc(f32x4 (&acc)[NF][2]) {
#pragma unroll
    for (int i = 0; i < NF; ++i) { acc[i][0] = f32x4{0, 0, 0, 0}; acc[i][1] = f32x4{0, 0, 0, 0}; }
}

constexpr int F_AB = 64 * 144, F_STAGE = F_AB + 8192;
struct FFrag { bf16x8 a[5], b[2]; };
DI void fread(FFrag& f, lptr pa, lptr pb) {
    f.b[0] = *(LAS bf16x8*)(pb); f.b[1] = *(LAS bf16x8*)(pb + 2048);
#pragma unroll
    for (int i = 0; i < 5; ++i) { const s16x4 lo = trread(pa + 32 * i), hi = trread(pa + 16 * 144 + 32 * i); f.a[i] = __builtin_shufflevector(lo, hi, 0, 1, 2, 3, 4, 5, 6, 7); }
}
template <int SL, int NWAIT, bool DOI>
DI void fstep(f32x4 (&acc)[5][2], FFrag& f, lptr pa, lptr pb, const char* sa, const char* sb, unsigned dA, unsigned dB, unsigned dX, const unsigned (&aoff)[3], unsigned bo0, unsigned bo1, int w) {
    wait_bar<NWAIT>();
    constexpr int SN = (SL + 1) & 3;
    FFrag n;
#pragma unroll
    for (int i = 0; i < 5; ++i) {
        if (DOI && i < 2) m0_set(dA, SL * F_STAGE + i * 4096);
        if (DOI && i >= 2 && i < 4) m0_set(dB, SL * F_STAGE + (i - 2) * 1024);
        if (i < 2) n.b[i] = *(LAS bf16x8*)(pb + SN * F_STAGE + i * 2048);
        SB();
        acc[i][0] = MFMA16(f.a[i], f.b[0], acc[i][0]);
        SB();
        if (DOI && i < 2) dma<false>(sa, aoff[i]);
        if (DOI && i >= 2 && i < 4) dma<true>(sb, i == 2 ? bo0 : bo1);
        { const s16x4 lo = trread(pa + SN * F_STAGE + 32 * i), hi = trread(pa + SN * F_STAGE + 16 * 144 + 32 * i); n.a[i] = __builtin_shufflevector(lo, hi, 0, 1, 2, 3, 4, 5, 6, 7); }
        SB();
        acc[i][1] = MFMA16(f.a[i], f.b[1], acc[i][1]);
        SB();
    }
    if (DOI && w == SL) { m0_set(dX, SL * F_STAGE); asm volatile("s_nop 0"); dma<false>(sa, aoff[2]); }
    f = n;
}
DI void final_aoff(unsigned (&aoff)[3], int w, int lane) {
#pragma unroll
    for (int n = 0; n < 3; ++n) {
        const int id = n < 2 ? (n * 4 + w) * 64 + lane : 512 + lane, rho = id / 9, c = id - rho * 9;
        aoff[n] = (unsigned)rho2k(rho) * (DM * 2) + (unsigned)c * 16u;
    }
}
DI void final_weights(const Params& P, unsigned lds0, int e, int k, int w, const unsigned (&aoff)[3]) {
    const char* src = (const char*)P.wout + 72 * e * 2 + (size_t)k * 64 * DM * 2;
    dma_now<false>(src, aoff[0], lds0 + k * F_STAGE + w * 1024);
    dma_now<false>(src, aoff[1], lds0 + k * F_STAGE + (4 + w) * 1024);
    if (w == k) dma_now<false>(src, aoff[2], lds0 + k * F_STAGE + 8192);
}
DI void final_tile(const Params& P, __amdgpu_buffer_rsrc_t rs, u64 tag, lptr lds, unsigned lds0, int tb, int e, int w, int lane) {
    const int d0 = 72 * e;
    const int th = w & 1, kh = w >> 1;
    int g = lane >> 4; const int q = (lane & 15) >> 2, p = lane & 3;
    char* ws = P.ws;
    unsigned aoff[3], boffo[2];
    final_aoff(aoff, w, lane);
    const unsigned bu0 = (unsigned)(w * 2048 + lane * 16), bu1 = bu0 + 1024;
    b_offsets_rowmajor<2>(boffo, 16 * w, lane, DM * 2);
    const lptr pa = lds + (32 * kh + 4 * g + q) * 144 + 8 * p;
    const lptr pb = lds + F_AB + (32 * th + (lane & 15)) * 128 + (((4 * kh + g) ^ (lane & 7)) * 16);
    const unsigned dA = lds0 + w * 1024, dB = lds0 + F_AB + w * 2048, dX = lds0 + 8192;
    f32x4 accb[5][2], acca[5][2];
    zero_acc<5>(accb); zero_acc<5>(acca);
    const char* wout = (const char*)P.wout + d0 * 2;
    const char* wo = (const char*)P.wo + d0 * 2;
    const char* ub = ws + WS_U + (size_t)tb * 36 * 8192;
    const char* obp = ws + WS_O + (size_t)tb * 64 * DM * 2;
    final_weights(P, lds0, e, 2, w, aoff);
    final_weights(P, lds0, e, 3, w, aoff);
    if (w == 0) await<2>(ws, WS_F3 + (unsigned)(((tb >> 1) * 4 + 2 * (tb & 1)) * 16 + (lane & 31)) * 8, tag);
    asm volatile("s_barrier" ::: "memory");
#pragma unroll
    for (int k = 0; k < 4; ++k) {
        dma_now<true>(ub + k * 8192, bu0, dB + k * F_STAGE);
        dma_now<true>(ub + k * 8192, bu1, dB + k * F_STAGE + 1024);
    }
    FFrag f;
    wait_bar<6>();
    fread(f, pa, pb);
#define FSRC(kn) ((kn) < 36 ? wout + (size_t)(kn) * 64 * DM * 2 : wo + (size_t)((kn) - 36) * 64 * DM * 2), ((kn) < 36 ? ub + (size_t)(kn) * 8192 : obp + ((kn) - 36) * 128)
#define FB0(kn) ((kn) < 36 ? bu0 : boffo[0]), ((kn) < 36 ? bu1 : boffo[1])
#define FSTEP(SL, NW, acc_, kn) fstep<SL, NW, true>(acc_, f, pa, pb, FSRC(kn), dA, dB, dX, aoff, FB0(kn), w)
    FSTEP(0, 4, accb, 4);
    FSTEP(1, 2 + 4, accb, 5);
    FSTEP(2, 8, accb, 6);
    FSTEP(3, 8, accb, 7);
    for (int k = 4; k < 32; k += 4) {
        fstep<0, 8, true>(accb, f, pa, pb, wout + (size_t)(k + 4) * 64 * DM * 2, ub + (size_t)(k + 4) * 8192, dA, dB, dX, aoff, bu0, bu1, w);
        fstep<1, 8, true>(accb, f, pa, pb, wout + (size_t)(k + 5) * 64 * DM * 2, ub + (size_t)(k + 5) * 8192, dA, dB, dX, aoff, bu0, bu1, w);
        fstep<2, 8, true>(accb, f, pa, pb, wout + (size_t)(k + 6) * 64 * DM * 2, ub + (size_t)(k + 6) * 8192, dA, dB, dX, aoff, bu0, bu1, w);
        fstep<3, 8, true>(accb, f, pa, pb, wout + (size_t)(k + 7) * 64 * DM * 2, ub + (size_t)(k + 7) * 8192, dA, dB, dX, aoff, bu0, bu1, w);
    }
    if (w == 0) await<4>(ws, WS_F4 + (unsigned)(tb * 32 + (lane & 31)) * 8, tag);
    asm volatile("s_barrier" ::: "memory");
    FSTEP(0, 8, accb, 36);
    FSTEP(1, 8, accb, 37);
    FSTEP(2, 8, accb, 38);
    FSTEP(3, 8, accb, 39);
    FSTEP(0, 8, acca, 40);
    FSTEP(1, 8, acca, 41);
    FSTEP(2, 8, acca, 42);
    FSTEP(3, 8, acca, 43);
    FSTEP(0, 8, acca, 44);
    fstep<1, 8, false>(acca, f, pa, pb, nullptr, nullptr, dA, dB, dX, aoff, 0, 0, w);
    fstep<2, 4, false>(acca, f, pa, pb, nullptr, nullptr, dA, dB, dX, aoff, 0, 0, w);
    fstep<3, 0, false>(acca, f, pa, pb, nullptr, nullptr, dA, dB, dX, aoff, 0, 0, w);
    fstep<0, 0, false>(acca, f, pa, pb, nullptr, nullptr, dA, dB, dX, aoff, 0, 0, w);
    lane = opq(lane); g = lane >> 4;
    float gt[2];
#pragma unroll
    for (int tt = 0; tt < 2; ++tt) gt[tt] = __uint_as_float(ld4(rs, WS_GATE + (unsigned)(tb * 64 + 32 * th + 16 * tt + (lane & 15)) * 4));
#pragma unroll
    for (int i = 0; i < 5; ++i)
#pragma unroll
        for (int tt = 0; tt < 2; ++tt) accb[i][tt] = acca[i][tt] * gt[tt] + accb[i][tt] * (1.0f - gt[tt]);
    WAIT_BAR0();
    const lptr xch = lds + th * 10240 + lane * 16;
    if (kh == 1) {
#pragma unroll
        for (int i = 0; i < 5; ++i)
#pragma unroll
            for (int tt = 0; tt < 2; ++tt) *(LAS f32x4*)(xch + (i * 2 + tt) * 1024) = accb[i][tt];
    }
    WAIT_BAR0();
    const lptr st = lds + 20480;
    if (kh == 0) {
#pragma unroll
        for (int i = 0; i < 5; ++i)
#pragma unroll
            for (int tt = 0; tt < 2; ++tt) {
                const f32x4 v = accb[i][tt] + *(LAS f32x4*)(xch + (i * 2 + tt) * 1024);
                u32x2 o; o[0] = pk2(v[0], v[1]); o[1] = pk2(v[2], v[3]);
                *(LAS u32x2*)(st + (32 * th + 16 * tt + (lane & 15)) * 160 + (16 * i + 4 * g) * 2) = o;
            }
    }
    WAIT_BAR0();
    for (int id = threadIdx.x; id < 64 * 9; id += 256) {
        const int row = id / 9, c = id - row * 9;
        *(u32x4*)((char*)P.out + (size_t)(tb * 64 + row) * (DM * 2) + d0 * 2 + c * 16) = *(LAS u32x4*)(st + row * 160 + c * 16);
    }
}

DI void role_g(const Params& P, u64 tag, lptr lds, unsigned lds0, int idx, int w, int lane0) {
    __builtin_amdgcn_s_setprio(1);
    const int x = idx & 7, y = idx >> 3;
    const int r = (x >> 1) * 4 + (y >> 3), j = (x & 1) * 8 + (y & 7);
    int lane = lane0;
    char* ws = P.ws;
    const __amdgpu_buffer_rsrc_t rs = ws_rsrc(ws);
    f32x4 acc[9][2];
    u32x4 z1[9];
    {
        Gemm<9, true> G; G.init(lds, lds0, w, lane);
        G.set_a(lane, 4608 * 2);
        b_offsets_rowmajor<4>(G.boff, 32 * w, lane, DM * 2);
        constexpr size_t WSUB = (size_t)32 * 4608 * 2;
        const char* wz2 = (const char*)P.win + (size_t)(FH + 144 * j) * 2;
        const char* wz1 = (const char*)P.win + (size_t)(144 * j) * 2;
        const char* xb = (const char*)P.ax + (size_t)r * 128 * DM * 2;
        G.pro_start(wz2, wz2 + WSUB, wz2 + 2 * WSUB, xb, xb + 128);
        G.pro_finish();
        zero_acc<9>(acc);
        {
            const char* a = wz2 + 3 * WSUB; const char* b = xb + 128;
            gemm_steady<0>(G, acc, a, WSUB, b, 0, 7);
            G.template kstep<0, 1>(acc, xb + 8 * 128, wz2 + 17 * WSUB, xb, wz1);
            G.template kstep<0, 0>(acc, xb, wz1 + WSUB, xb + 128, wz1 + 2 * WSUB);
        }
        lane = opq(lane0);
        u32x4 zp[9];
        {
            float mean[2], m2[2];
#pragma unroll
            for (int tt = 0; tt < 2; ++tt) {
                f32x2 y[9][2], s2 = {0.f, 0.f};
#pragma unroll
                for (int i = 0; i < 9; ++i) {
                    y[i][0] = gelu2(f32x2{acc[i][tt][0], acc[i][tt][1]}); y[i][1] = gelu2(f32x2{acc[i][tt][2], acc[i][tt][3]});
                    s2 += y[i][0] + y[i][1];
                    zp[i][2 * tt] = pk2v(y[i][0]); zp[i][2 * tt + 1] = pk2v(y[i][1]);
                }
                mean[tt] = red32(red16(s2.x + s2.y)) * (1.0f / 144.0f);
                const f32x2 mv = {mean[tt], mean[tt]};
                f32x2 q2 = {0.f, 0.f};
#pragma unroll
                for (int i = 0; i < 9; ++i) {
                    const f32x2 d0 = y[i][0] - mv, d1 = y[i][1] - mv;
                    q2 = __builtin_elementwise_fma(d0, d0, q2); q2 = __builtin_elementwise_fma(d1, d1, q2);
                }
                m2[tt] = red32(red16(q2.x + q2.y));
            }
            if (lane < 32) {
                const int tt = lane >> 4;
                const float mu = tt ? mean[1] : mean[0], mm = tt ? m2[1] : m2[0];
                stf(ws, WS_XBUF + (unsigned)((r * 16 + j) * 128 + 32 * w + lane) * 8, ((u64)__float_as_uint(mm) << 32) | __float_as_uint(mu));
            }
            publish(ws, WS_F1 + (unsigned)((r * 4 + w) * 16 + j) * 8, tag, lane);
        }
        await<1>(ws, WS_F1 + (unsigned)((r * 4 + w) * 16 + (lane & 15)) * 8, tag);
        {
            const int g = lane >> 4;
            const unsigned xo = WS_XBUF + (unsigned)((r * 16 + (lane >> 5) * 8) * 128 + 32 * w + (lane & 31)) * 8;
            float m[8], q2[8], sm = 0.f;
#pragma unroll
            for (int e = 0; e < 8; ++e) { const u64 v = ldf(ws, xo + e * 1024); m[e] = __uint_as_float((unsigned)v); q2[e] = __uint_as_float((unsigned)(v >> 32)); sm += m[e]; }
            const float mean = red32(sm) * (1.0f / 16.0f);
            float qq = 0.f;
#pragma unroll
            for (int e = 0; e < 8; ++e) { const float d = m[e] - mean; qq += q2[e] + 144.0f * d * d; }
            qq = red32(qq);
            const float rstd = 1.0f / sqrtf(qq * (1.0f / 2304.0f) + 1e-5f);
            float ra[2], rb[2];
#pragma unroll
            for (int tt = 0; tt < 2; ++tt) { ra[tt] = __shfl(rstd, 16 * tt + (lane & 15)); rb[tt] = -__shfl(mean, 16 * tt + (lane & 15)) * ra[tt]; }
            const lptr st = lds + 69632 + w * 3072;
            const lptr rd = st + (4 * g + ((lane & 15) >> 2)) * 96 + 8 * (lane & 3);
            const unsigned dst = WS_Z2N + (unsigned)(j * 64 + 4 * r + w) * 9216 + lane * 16;
#pragma unroll
            for (int p3 = 0; p3 < 3; ++p3) {
#pragma unroll
                for (int tt = 0; tt < 2; ++tt) {
                    const int gp = 2 * tt + ((lane & 15) >> 3), h = (lane >> 2) & 1, ii = lane & 3;
                    const lptr rowp = st + (16 * h + 4 * gp + ii) * 96 + 8 * g;
#pragma unroll
                    for (int c3 = 0; c3 < 3; ++c3) {
                        const int i = 3 * p3 + c3;
                        u32x2 v;
                        const f32x2 a2 = {ra[tt], ra[tt]}, b2 = {rb[tt], rb[tt]};
                        v[0] = pk2v(__builtin_elementwise_fma(f32x2{bflo(zp[i][2 * tt]), bfhi(zp[i][2 * tt])}, a2, b2));
                        v[1] = pk2v(__builtin_elementwise_fma(f32x2{bflo(zp[i][2 * tt + 1]), bfhi(zp[i][2 * tt + 1])}, a2, b2));
                        *(LAS u32x2*)(rowp + 32 * c3) = v;
                    }
                }
                LGKM0();
#pragma unroll
                for (int c3 = 0; c3 < 3; ++c3) {
                    const s16x4 lo = trread(rd + 32 * c3), hi = trread(rd + 16 * 96 + 32 * c3);
                    st16(rs, dst + (3 * p3 + c3) * 1024, __builtin_bit_cast(u32x4, __builtin_shufflevector(lo, hi, 0, 1, 2, 3, 4, 5, 6, 7)));
                }
                LGKM0();
            }
        }
#define ZK(XT, kk) G.template kstep<0, ((kk) + 9) & 1, XT>(acc, xb + ((kk) + 1) * 128, wz1 + (2 * (kk) + 3) * WSUB, xb + ((kk) + 2) * 128, wz1 + (2 * (kk) + 4) * WSUB)
        zero_acc<9>(acc);
        ZK(9, 0);
        ZK(0, 1);
        lane = opq(lane0);
        if (lane == 0) stf(ws, WS_F2 + (unsigned)(j * 64 + 4 * r + w) * 8, tag);
        ZK(1, 2); ZK(0, 3); ZK(0, 4); ZK(0, 5); ZK(0, 6);
        G.template kstep<1, 0>(acc, xb + 8 * 128, wz1 + 17 * WSUB, nullptr, nullptr);
        G.template kstep<2, 1>(acc, nullptr, nullptr, nullptr, nullptr);
    }
#pragma unroll
    for (int i = 0; i < 9; ++i)
        z1[i] = u32x4{pk2v(gelu2(f32x2{acc[i][0][0], acc[i][0][1]})), pk2v(gelu2(f32x2{acc[i][0][2], acc[i][0][3]})),
                      pk2v(gelu2(f32x2{acc[i][1][0], acc[i][1][1]})), pk2v(gelu2(f32x2{acc[i][1][2], acc[i][1][3]}))};
    u16 bsv[2];
#pragma unroll
    for (int tt = 0; tt < 2; ++tt) bsv[tt] = P.bsp[r * 128 + 32 * w + 16 * tt + (lane & 15)];
    {
        Gemm<9, false> G; G.init(lds, lds0, w, lane);
        G.set_a(lane, 0);
        b_offsets_rowmajor<4>(G.boff, 32 * w, lane, SEQ * 2);
        const char* zb = ws + WS_Z2N + (size_t)j * 64 * 9216;
        const char* wb = (const char*)P.wsp + (size_t)r * 128 * SEQ * 2;
        if (w == 0) await<2>(ws, WS_F2 + (unsigned)(j * 64 + lane) * 8, tag);
        WAIT_BAR0();
        __builtin_amdgcn_s_setprio(0);
        G.pro_start(zb, zb + 9216, zb + 2 * 9216, wb, wb + 128);
        G.pro_finish();
        zero_acc<9>(acc);
        const char* a = zb + 3 * 9216; const char* b = wb + 128;
        gemm_steady<0>(G, acc, a, (size_t)9216, b, 0, 30);
        G.template kstep<1, 0>(acc, b, a, nullptr, nullptr);
        G.template kstep<2, 1>(acc, nullptr, nullptr, nullptr, nullptr);
    }
    lane = opq(lane0);
    const int tbf = 2 * r + (j >> 3);
    {
        const int g = lane >> 4;
        float bs[2];
#pragma unroll
        for (int tt = 0; tt < 2; ++tt) bs[tt] = __uint_as_float((unsigned)bsv[tt] << 16);
        WAIT_BAR0();
        {
            unsigned fa[3];
            final_aoff(fa, w, lane);
            final_weights(P, lds0, j & 7, 0, w, fa);
            final_weights(P, lds0, j & 7, 1, w, fa);
        }
        const lptr st = lds + 45056 + w * 9216;
#pragma unroll
        for (int i = 0; i < 9; ++i)
#pragma unroll
            for (int tt = 0; tt < 2; ++tt) {
                u32x2 v;
                const f32x2 b2 = {bs[tt], bs[tt]};
                v[0] = pk2v(f32x2{bflo(z1[i][2 * tt]), bfhi(z1[i][2 * tt])} * (f32x2{acc[i][tt][0], acc[i][tt][1]} + b2));
                v[1] = pk2v(f32x2{bflo(z1[i][2 * tt + 1]), bfhi(z1[i][2 * tt + 1])} * (f32x2{acc[i][tt][2], acc[i][tt][3]} + b2));
                *(LAS u32x2*)(st + (16 * tt + (lane & 15)) * 288 + (16 * i + 4 * g) * 2) = v;
            }
        LGKM0();
        const int tb = 2 * r + (w >> 1);
#pragma unroll
        for (int n = 0; n < 9; ++n) {
            const int id = n * 64 + lane, row = id / 18, c = id - row * 18;
            const int cg = 18 * j + c, t64 = 32 * (w & 1) + row;
            st16(rs, WS_U + (unsigned)(tb * 36 + (cg >> 3)) * 8192 + t64 * 128 + (((cg & 7) ^ (t64 & 7)) * 16), *(LAS u32x4*)(st + row * 288 + c * 16));
        }
        publish(ws, WS_F3 + (unsigned)((r * 4 + w) * 16 + j) * 8, tag, lane);
        asm volatile("s_barrier" ::: "memory");
    }
    lane = opq(lane0);
    final_tile(P, rs, tag, lds, lds0, tbf, j & 7, w, lane);
}

constexpr int AT_CONST = 3 * AT_CH;
DI void attn_issue(const char* kv, int ci, unsigned ldsb, int w, int lane) {
    const char* src = kv + (size_t)ci * AT_CH;
#pragma unroll
    for (int n = 0; n < 5; ++n) dma_now<true>(src, (unsigned)((n * 4 + w) * 1024 + lane * 16), ldsb + (n * 4 + w) * 1024);
    if (w == (ci & 3)) dma_now<true>(src, (unsigned)(20480 + lane * 16), ldsb + 20480);
}
#define ZERO16 (f32x16{0, 0, 0, 0, 0, 0, 0, 0, 0, 0, 0, 0, 0, 0, 0, 0})
#define BF8(x) __builtin_bit_cast(bf16x8, x)
#define GRP(S, o_, Pv, j_) do { SB(); unsigned pa_, pb_; float t0_, t1_, t2_, t3_; \
    asm volatile("v_exp_f32 %2, %6\n\tv_exp_f32 %3, %7\n\tv_exp_f32 %4, %8\n\tv_exp_f32 %5, %9\n\tv_cvt_pk_bf16_f32 %0, %2, %3\n\tv_cvt_pk_bf16_f32 %1, %4, %5" \
        : "=&v"(pa_), "=&v"(pb_), "=&v"(t0_), "=&v"(t1_), "=&v"(t2_), "=&v"(t3_) : "v"(S[o_]), "v"(S[o_ + 1]), "v"(S[o_ + 2]), "v"(S[o_ + 3])); \
    Pv[j_] = pa_; Pv[j_ + 1] = pb_; SB(); } while (0)
DI void attn_step(f32x16 (&o)[2], f32x16& sA, u32x4& p1, bf16x8& kf1, bf16x8& kf2, bf16x8& vf1, const bf16x8 (&qf1)[2], const u32x4 (&qf2)[2],
                  lptr& pv, unsigned vstep, lptr pk1n  , lptr pk2n  ) {
    f32x16 sB; u32x4 p0;
    sB = MFMA32(kf1, qf1[1], ZERO16);
    const bf16x8 vf0 = *(LAS bf16x8*)(pv); pv += vstep;      GRP(sA, 0, p0, 0);
    o[1] = MFMA32(vf1, BF8(p1), o[1]);
    const bf16x8 nk1 = *(LAS bf16x8*)(pk1n);                 GRP(sA, 4, p0, 2);
    sB = MFMA32(kf2, BF8(qf2[1]), sB);
    const bf16x8 nv1 = *(LAS bf16x8*)(pv); pv += vstep;      GRP(sA, 8, p1, 0);
    o[0] = MFMA32(vf0, BF8(p0), o[0]);
    const bf16x8 nk2 = *(LAS bf16x8*)(pk2n);                 GRP(sA, 12, p1, 2);
    sA = MFMA32(nk1, qf1[0], ZERO16);                        GRP(sB, 0, p0, 0);
    o[0] = MFMA32(nv1, BF8(p1), o[0]);                       GRP(sB, 4, p0, 2);
    sA = MFMA32(nk2, BF8(qf2[0]), sA);                       GRP(sB, 8, p1, 0);
    o[1] = MFMA32(vf0, BF8(p0), o[1]);                       GRP(sB, 12, p1, 2);
    kf1 = nk1; kf2 = nk2; vf1 = nv1;
}
struct Prefetch {
    const char* ws; const char* zq;
    const char* wq;
    unsigned f2; u64 tag, seen;
    int k;
    unsigned lds0;
    DI void step(int ci, int w, int lane) {
        if (ci < 4 && w < 2) touch(lds0, wq + (size_t)(lane & 15) * (SEQ * 2) + (ci * 8 + w * 4 + (lane >> 4)) * 128);
        if (k < 0) {
            if (__builtin_amdgcn_readfirstlane(__all(seen == tag))) k = 0; else seen = ldf(ws, f2);
        }
        if (k >= 0 && k < 32) {
            const int kk = k + lane / 9;
            touch_sc1(lds0, zq + (size_t)(kk < 31 ? kk : 31) * 18432 + (lane % 9) * 128);
            k += (k == 0) ? 7 : 5;
        }
    }
};
template <bool PF>
DI void attn_sweep(f32x16 (&o)[2], const bf16x8 (&qf1)[2], const u32x4 (&qf2)[2], const char* kv, lptr lds, unsigned lds0, int w, int lane, Prefetch& pf) {
    const int row = lane & 31, hh = lane >> 5;
    const lptr pk1 = lds + lane * 16, pk2 = lds + AT_K2 + row * 16;
    const unsigned vmul = row < 18 ? 1u : 0u, vstep = vmul * 576u;
    const lptr vbase = row < 18 ? lds + AT_V + hh * 288 + row * 16 : lds + AT_CONST + (row == 18 ? 0 : 16);
    wait_bar<5>();
    bf16x8 kf1 = *(LAS bf16x8*)(pk1), kf2 = *(LAS bf16x8*)(pk2), vf1 = kf1;
    f32x16 sA = MFMA32(kf1, qf1[0], ZERO16);
    sA = MFMA32(kf2, BF8(qf2[0]), sA);
    asm volatile("s_nop 7\n\ts_nop 7" : "+v"(sA));
    u32x4 p1 = {0u, 0u, 0u, 0u};
#pragma unroll
    for (int b = 0; b < 2; ++b) o[b] = ZERO16;
    unsigned cur = 0;
    for (int ci = 0; ci < 8; ++ci) {
        const unsigned nxt = (cur == 2 * AT_CH) ? 0u : cur + AT_CH;
        lptr pv = vbase + vmul * cur;
#pragma unroll
        for (int kb = 0; kb < 7; ++kb) attn_step(o, sA, p1, kf1, kf2, vf1, qf1, qf2, pv, vstep, pk1 + cur + (kb + 1) * 1024, pk2 + cur + (kb + 1) * 512);
        wait_bar<0>();
        if (ci + 2 < 8) attn_issue(kv, ci + 2, lds0 + (nxt == 2 * AT_CH ? 0u : nxt + AT_CH), w, lane);
        if (PF) pf.step(ci + 1, w, lane);
        const unsigned kn = (ci + 1 < 8) ? nxt : cur;
        attn_step(o, sA, p1, kf1, kf2, vf1, qf1, qf2, pv, vstep, pk1 + kn, pk2 + kn);
        cur = nxt;
    }
    asm volatile("s_nop 1" : "+v"(p1));
    o[1] = MFMA32(vf1, BF8(p1), o[1]);
    asm volatile("" : "+v"(o[0]), "+v"(o[1]), "+v"(sA));
}
DI void attn_rowmax(float (&mx)[2], const bf16x8 (&qf1)[2], const u32x4 (&qf2)[2], const char* kv, lptr lds, unsigned lds0, int w, int lane) {
    mx[0] = -INFINITY; mx[1] = -INFINITY;
#pragma unroll 1
    for (int ci = 0; ci < 8; ++ci) {
        WAIT_BAR0();
        attn_issue(kv, ci, lds0, w, lane);
        WAIT_BAR0();
#pragma unroll 1
        for (int kb = 0; kb < 8; ++kb) {
            const bf16x8 kf1 = *(LAS bf16x8*)(lds + lane * 16 + kb * 1024), kf2 = *(LAS bf16x8*)(lds + AT_K2 + (lane & 31) * 16 + kb * 512);
#pragma unroll
            for (int b = 0; b < 2; ++b) {
                f32x16 s = MFMA32(kf1, qf1[b], ZERO16);
                s = MFMA32(kf2, BF8(qf2[b]), s);
#pragma unroll
                for (int e = 0; e < 16; ++e) mx[b] = fmaxf(mx[b], s[e]);
            }
        }
    }
    WAIT_BAR0();
}
DI void role_a(const Params& P, u64 tag, lptr lds, unsigned lds0, int idx, int w, int lane) {
    const int x = idx & 7, y = idx >> 3;
    char* ws = P.ws;
    const __amdgpu_buffer_rsrc_t rs = ws_rsrc(ws);
    const int row = lane & 31, hh = lane >> 5;
    {
        const int r = (x >> 1) * 4 + (y >> 3), c = (x & 1) * 8 + (y & 7);
        const int col0 = (108 * c) & ~7, shift = 108 * c - col0;
        constexpr size_t QSUB = (size_t)32 * 1728 * 2;
        const char* wq = (const char*)P.wqkv + (size_t)col0 * 2;
        const char* xb = (const char*)P.ax + (size_t)r * 128 * DM * 2;
        const int blk = 4 * r + w;
        f32x4 acc[7][2];
        zero_acc<7>(acc);
#define TOUCH_Z1() do { const int t = threadIdx.x, rq = (r & 3) * 144; _Pragma("unroll") for (int n = 0; n < 3; ++n) { \
            const int id = n * 256 + t, row_ = id < 576 ? id >> 2 : 0, o = (id & 3) == 3 ? 284 : (id & 3) * 128; \
            touch(lds0, (const char*)P.win + (size_t)(rq + row_) * (4608 * 2) + (144 * c) * 2 + o); } } while (0)
#define WARM2() do { const int t = threadIdx.x; touch(lds0, (const char*)P.wsp + (size_t)(idx * 256 + t) * 128); \
            if (w < 2) { const bool o_ = idx >= 204; const int ln = (o_ ? idx - 204 : idx) * 102 + (t < 102 ? t : 0); \
                if (o_) touch(lds0, (const char*)P.wo + (size_t)(ln < 5184 ? ln : 0) * 128); else touch(lds0, (const char*)P.wout + (size_t)(ln < 20736 ? ln : 0) * 128); } } while (0)
#define QKV_LOOP(G) do { G.init(lds, lds0, w, lane); G.set_a(lane, 1728 * 2); b_offsets_rowmajor<4>(G.boff, 32 * w, lane, DM * 2); \
            G.pro_start(wq, wq + QSUB, wq + 2 * QSUB, xb, xb + 128); G.pro_finish(); \
            const char* a_ = wq + 3 * QSUB; const char* b_ = xb + 128; \
            gemm_steady<0>(G, acc, a_, QSUB, b_, 0, 7); \
            G.template kstep<1, 1>(acc, b_, a_, nullptr, nullptr); \
            G.template kstep<2, 0>(acc, nullptr, nullptr, nullptr, nullptr); WARM2(); \
            asm volatile("s_waitcnt lgkmcnt(0)\n\ts_barrier" ::: "memory"); } while (0)
        if (c >= 11) {
            Gemm<7, true, true> G;
            QKV_LOOP(G);
            const int ln = opq(lane), g = ln >> 4;
            const lptr V = lds + w * 6912;
#pragma unroll
            for (int i = 0; i < 7; ++i) {
                const int n = 16 * i + (ln & 15) - shift, uu = n / 18, d = n - 18 * uu;
                if (n >= 0 && n < 108) {
#pragma unroll
                    for (int tt = 0; tt < 2; ++tt)
                        *(LAS u32x2*)(V + uu * 1152 + tt * 576 + (g & 1) * 288 + d * 16 + (g >> 1) * 8) = u32x2{pk2(acc[i][tt][0], acc[i][tt][1]), pk2(acc[i][tt][2], acc[i][tt][3])};
                }
            }
            LGKM0();
#pragma unroll
            for (int n = 0; n < 7; ++n) {
                const int id = n * 64 + ln, uu = id / 72, rest = id - uu * 72;
                if (id < 432) st16(rs, WS_KV + (unsigned)(((6 * c + uu) & 31) * 8 + (blk >> 3)) * AT_CH + AT_V + (blk & 7) * 1152 + rest * 16, *(LAS u32x4*)(V + id * 16));
            }
        } else {
            Gemm<7, true> G;
            QKV_LOOP(G);
            const int ln = opq(lane), g = ln >> 4, row = ln & 31, hh = ln >> 5;
            constexpr int TP = 232;
            constexpr float QS = 0.23570226039551584f * 1.4426950408889634f;
            const lptr T = lds + w * (32 * TP);
#pragma unroll
            for (int i = 0; i < 7; ++i) {
                float sc[4];
                if (col0 + 16 * i + 16 <= 576) { sc[0] = sc[1] = sc[2] = sc[3] = QS; }
                else if (col0 + 16 * i >= 576) { sc[0] = sc[1] = sc[2] = sc[3] = 1.0f; }
                else {
#pragma unroll
                    for (int e = 0; e < 4; ++e) sc[e] = (col0 + 16 * i + 4 * g + e < 576) ? QS : 1.0f;
                }
#pragma unroll
                for (int tt = 0; tt < 2; ++tt) {
                    u32x2 v; v[0] = pk2(acc[i][tt][0] * sc[0], acc[i][tt][1] * sc[1]); v[1] = pk2(acc[i][tt][2] * sc[2], acc[i][tt][3] * sc[3]);
                    *(LAS u32x2*)(T + (16 * tt + (ln & 15)) * TP + (16 * i + 4 * g) * 2) = v;
                }
            }
            LGKM0();
            u32x4 fv[6]; unsigned fe[6];
#pragma unroll
            for (int uu = 0; uu < 6; ++uu) {
                const int n0 = shift + 18 * uu;
                const lptr src = T + row * TP + (n0 + 8 * hh) * 2;
#pragma unroll
                for (int e = 0; e < 4; ++e) fv[uu][e] = *(LAS unsigned*)(src + 4 * e);
                fe[uu] = *(LAS unsigned*)(T + row * TP + (n0 + 16) * 2);
            }
            asm volatile("" :: "v"(fv[5]), "v"(fe[5]));
#pragma unroll
            for (int uu = 0; uu < 6; ++uu) {
                const int u = 6 * c + uu, type = u >> 5, h = u & 31, n0 = shift + 18 * uu;
                const unsigned kvb = WS_KV + (unsigned)(h * 8 + (blk >> 3)) * AT_CH;
                const u32x4 v = fv[uu]; const unsigned ex = fe[uu];
                if (type == 0) {
                    st16(rs, WS_QF1 + (unsigned)(h * 64 + blk) * 1024 + ln * 16, v);
                    if (hh == 0) st4(rs, WS_Q2 + (unsigned)(h * SEQ + blk * 32 + row) * 4, ex);
                } else if (type == 1) {
                    st16(rs, kvb + (blk & 7) * 1024 + ln * 16, v);
                    if (hh == 0) st16(rs, kvb + AT_K2 + ((blk & 7) * 32 + row) * 16, u32x4{ex, 0x3f803f80u, 0x00003f80u, 0u});
                    float s = 0.f;
#pragma unroll
                    for (int e = 0; e < 4; ++e) s += bflo(v[e]) * bflo(v[e]) + bfhi(v[e]) * bfhi(v[e]);
                    if (hh == 0) s += bflo(ex) * bflo(ex) + bfhi(ex) * bfhi(ex);
                    s = red32(s);
                    s = fmaxf(s, __builtin_bit_cast(float, __builtin_amdgcn_update_dpp(0, __builtin_bit_cast(int, s), 0xB1, 0xF, 0xF, true)));
                    s = fmaxf(s, __builtin_bit_cast(float, __builtin_amdgcn_update_dpp(0, __builtin_bit_cast(int, s), 0x4E, 0xF, 0xF, true)));
                    s = fmaxf(s, __builtin_bit_cast(float, __builtin_amdgcn_update_dpp(0, __builtin_bit_cast(int, s), 0x141, 0xF, 0xF, true)));
                    s = fmaxf(s, __builtin_bit_cast(float, __builtin_amdgcn_update_dpp(0, __builtin_bit_cast(int, s), 0x140, 0xF, 0xF, true)));
                    { auto rr = __builtin_amdgcn_permlane16_swap(__float_as_uint(s), __float_as_uint(s), false, false); s = fmaxf(__uint_as_float(rr[0]), __uint_as_float(rr[1])); }
                    if (ln == 0) st4(rs, WS_KMAX + (unsigned)(h * 64 + blk) * 4, __float_as_uint(s));
                } else if (row < 18) {
#pragma unroll
                    for (int s = 0; s < 2; ++s) {
                        u32x4 vv;
#pragma unroll
                        for (int e = 0; e < 4; ++e) {
                            const int k0 = 16 * s + 8 * (e >> 1) + 4 * hh + 2 * (e & 1);
                            const unsigned lo = *(LAS u16*)(T + k0 * TP + (n0 + row) * 2), hi = *(LAS u16*)(T + (k0 + 1) * TP + (n0 + row) * 2);
                            vv[e] = lo | (hi << 16);
                        }
                        st16(rs, kvb + AT_V + (blk & 7) * 1152 + s * 576 + hh * 288 + row * 16, vv);
                    }
                }
            }
        }
        publish(ws, WS_F5 + (unsigned)(c * 64 + blk) * 8, tag, lane);
        TOUCH_Z1();
    }
    const int h = x * 4 + (y >> 3), qc = y & 7;
    const char* kv = ws + WS_KV + (size_t)h * 8 * AT_CH;
    {
        const int grow = qc * 256 + w * 64 + 2 * h + hh;
        float s = 0.f;
#pragma unroll
        for (int n = 0; n < 3; ++n) {
            const int c = row + 32 * n;
            if (c < 72) {
                const u32x4 xv = *(const u32x4*)(P.ax + (size_t)grow * DM + c * 8), gv = *(const u32x4*)(P.gw + c * 8);
#pragma unroll
                for (int e = 0; e < 4; ++e) s += bflo(xv[e]) * bflo(gv[e]) + bfhi(xv[e]) * bfhi(gv[e]);
            }
        }
#pragma unroll
        for (int o = 16; o >= 1; o >>= 1) s += __shfl_xor(s, o);
        if (row == 0) st4(rs, WS_GATE + grow * 4, __float_as_uint(__builtin_amdgcn_rcpf(1.0f + __builtin_amdgcn_exp2f(-1.4426950408889634f * s))));
    }
    if (w == 0) {
        const unsigned fq = WS_F5 + (unsigned)((h / 6) * 64 + lane) * 8, fk = WS_F5 + (unsigned)(((32 + h) / 6) * 64 + lane) * 8, fv = WS_F5 + (unsigned)(((64 + h) / 6) * 64 + lane) * 8;
        for (unsigned spins = 0; spins < (1u << 16); ++spins) {
            const u64 a = ldf(ws, fq), b = ldf(ws, fk), c = ldf(ws, fv);
            if (__all(a == tag && b == tag && c == tag)) break;
            __builtin_amdgcn_s_sleep(6);
        }
    }
    if (threadIdx.x < 2) { const unsigned v = threadIdx.x == 0 ? 0x3f803f80u : 0u; *(LAS u32x4*)(lds + AT_CONST + threadIdx.x * 16) = u32x4{v, v, v, v}; }
    asm volatile("s_waitcnt lgkmcnt(0)\n\ts_barrier" ::: "memory");
    const unsigned kmraw = ld4(rs, WS_KMAX + (unsigned)(h * 64 + lane) * 4);
    u32x4 qv[2]; unsigned qe[2];
#pragma unroll
    for (int b = 0; b < 2; ++b) {
        const int qb = qc * 8 + w * 2 + b;
        qv[b] = ld16(rs, WS_QF1 + (unsigned)(h * 64 + qb) * 1024 + lane * 16);
        qe[b] = ld4(rs, WS_Q2 + (unsigned)(h * SEQ + qb * 32 + row) * 4);
    }
    attn_issue(kv, 0, lds0, w, lane);
    attn_issue(kv, 1, lds0 + AT_CH, w, lane);
    float kmax2 = __uint_as_float(kmraw);
#pragma unroll
    for (int o = 32; o >= 1; o >>= 1) kmax2 = fmaxf(kmax2, __shfl_xor(kmax2, o));
    bf16x8 qf1[2]; u32x4 qf2[2];
#pragma unroll
    for (int b = 0; b < 2; ++b) {
        const u32x4 v = qv[b];
        const unsigned ex = qe[b];
        float s = 0.f;
#pragma unroll
        for (int e = 0; e < 4; ++e) s += bflo(v[e]) * bflo(v[e]) + bfhi(v[e]) * bfhi(v[e]);
        if (hh == 0) s += bflo(ex) * bflo(ex) + bfhi(ex) * bfhi(ex);
        s = red32(s);
        const float M = sqrtf(s * kmax2) * 1.0078125f;
        const unsigned mb = ((__float_as_uint(M) + 0xffffu) >> 16) | 0x8000u;
        qf1[b] = __builtin_bit_cast(bf16x8, v);
        qf2[b] = hh == 0 ? u32x4{ex, mb, 0u, 0u} : u32x4{0u, 0u, 0u, 0u};
    }
    f32x16 o[2];
    Prefetch pf;
    {
        const int r = (x >> 1) * 4 + (y >> 3), j = (x & 1) * 8 + (y & 7);
        pf.ws = ws; pf.zq = ws + WS_Z2N + (size_t)j * 64 * 9216 + ((r & 3) * 36 + w * 9) * 128;
        pf.wq = (const char*)P.wsp + (size_t)(r * 128 + (j & 7) * 16) * (SEQ * 2);
        pf.f2 = WS_F2 + (unsigned)(j * 64 + lane) * 8; pf.tag = tag; pf.seen = tag + 1; pf.lds0 = lds0;
        pf.k = -1;
        pf.step(0, w, lane);
    }
    attn_sweep<true>(o, qf1, qf2, kv, lds, lds0, w, lane, pf);
    {
        bool bad = false;
#pragma unroll
        for (int b = 0; b < 2; ++b) { const float l = __shfl(o[b][10], row); bad |= !(l >= 8.271806125530277e-25f); }
        LAS unsigned* vote = (LAS unsigned*)(lds + AT_CONST + 32);
        if (threadIdx.x == 0) *vote = 0u;
        WAIT_BAR0();
        if (__any(bad) && lane == 0) *vote = 1u;
        WAIT_BAR0();
        if (__builtin_amdgcn_readfirstlane(*vote) != 0u) {
#pragma unroll
            for (int b = 0; b < 2; ++b) qf2[b][1] = 0u;
            float mx[2];
            attn_rowmax(mx, qf1, qf2, kv, lds, lds0, w, lane);
#pragma unroll
            for (int b = 0; b < 2; ++b) {
                const float nm = -max32(mx[b]);
                const unsigned h1 = __float_as_uint(nm) & 0xffff0000u; const float r1 = nm - __uint_as_float(h1);
                const unsigned h2 = __float_as_uint(r1) & 0xffff0000u; const float r2 = r1 - __uint_as_float(h2);
                const unsigned h3 = __float_as_uint(r2) & 0xffff0000u;
                if (hh == 0) { qf2[b][1] = (h1 >> 16) | h2; qf2[b][2] = h3 >> 16; }
            }
            attn_issue(kv, 0, lds0, w, lane);
            attn_issue(kv, 1, lds0 + AT_CH, w, lane);
            attn_sweep<false>(o, qf1, qf2, kv, lds, lds0, w, lane, pf);
            WAIT_BAR0();
        }
    }
    const lptr st = lds + w * 2560;
#pragma unroll
    for (int b = 0; b < 2; ++b) {
        const float inv = 1.0f / __shfl(o[b][10], row);
        const lptr rp = st + (32 * b + row) * 36 + hh * 8;
        *(LAS unsigned*)(rp + 0) = pk2(o[b][0] * inv, o[b][1] * inv);
        *(LAS unsigned*)(rp + 4) = pk2(o[b][2] * inv, o[b][3] * inv);
        *(LAS unsigned*)(rp + 16) = pk2(o[b][4] * inv, o[b][5] * inv);
        *(LAS unsigned*)(rp + 20) = pk2(o[b][6] * inv, o[b][7] * inv);
        if (hh == 0) *(LAS unsigned*)(rp + 32) = pk2(o[b][8] * inv, o[b][9] * inv);
    }
    LGKM0();
#pragma unroll
    for (int n = 0; n < 9; ++n) {
        const int id = n * 64 + lane, rr = id / 9, cc = id - rr * 9;
        st4(rs, WS_O + (unsigned)(qc * 256 + w * 64 + rr) * (DM * 2) + h * 36 + cc * 4, *(LAS unsigned*)(st + id * 4));
    }
    publish(ws, WS_F4 + (unsigned)((qc * 4 + w) * 32 + h) * 8, tag, lane);
    if (idx == 0 && w == 0) {
#pragma unroll 1
        for (int n = 0; n < 4; ++n) {
            await(ws, WS_F1 + (unsigned)(((4 * n + (lane >> 4)) * 4) * 16 + (lane & 15)) * 8, tag);
            await(ws, WS_F5 + (unsigned)((4 * n + (lane >> 4)) * 64 + 4 * (lane & 15)) * 8, tag);
        }
        if (lane == 0) stf(ws, WS_NONCE, tag);
    }
    VM0();
#pragma unroll 1
    for (int n = 0; n < 64 && pf.k < 32; ++n) {
        pf.step(8, w, lane);
        VM0();
        __builtin_amdgcn_s_sleep(32);
    }
    {
        const int t = threadIdx.x;
        const int r = (x >> 1) * 4 + (y >> 3), j = (x & 1) * 8 + (y & 7), tb = 2 * r + (j >> 3);
#define WTOUCH(id_) do { const int id = (id_) < 810 ? (id_) : 0, ks = id / 18, ln = 18 * y + id % 18; \
            touch(lds0, ks < 36 ? (const char*)P.wout + (size_t)(576 * ks + ln) * 128 : (const char*)P.wo + (size_t)(576 * (ks - 36) + ln) * 128); } while (0)
        await<16>(ws, WS_F3 + (unsigned)((r * 4 + 2 * (tb & 1)) * 16 + (lane & 31)) * 8, tag);
        WTOUCH(t);
        const char* ub = ws + WS_U + (size_t)tb * 36 * 8192 + (size_t)(j & 7) * 8 * 128;
        touch_sc1(lds0, ub + (size_t)(t >> 3) * 8192 + (t & 7) * 128);
        touch_sc1(lds0, ub + (size_t)(32 + ((t >> 3) & 3)) * 8192 + (t & 7) * 128);
        WTOUCH(256 + t);
        VM0();
        __builtin_amdgcn_s_sleep(48);
        WTOUCH(512 + t); WTOUCH(768 + t);
        await<16>(ws, WS_F4 + (unsigned)(tb * 32 + (lane & 31)) * 8, tag);
        { const int id = (t < 72 ? t : 0) + (j & 7) * 72; touch_sc1(lds0, ws + WS_O + (size_t)(tb * 64 + id / 9) * (DM * 2) + (id % 9) * 128); }
        VM0();
    }
}

__global__ __launch_bounds__(256, 2) void gated_mix_kernel(Params P) {
    extern __shared__ __attribute__((aligned(16))) char smem[];
    const lptr lds = (lptr)smem;
    const unsigned lds0 = (unsigned)(uintptr_t)lds;
    const int w = __builtin_amdgcn_readfirstlane(threadIdx.x >> 6), lane = threadIdx.x & 63;
    const u64 tag = ldf(P.ws, WS_NONCE) + 1ull;
    if (w < 3) {
        const int b = blockIdx.x, t = threadIdx.x < 148 ? threadIdx.x : 0;
        const char* base; int first, count;
        if (b < 125) { base = (const char*)P.ax; first = b * 148; count = 18432; }
        else if (b < 406) { base = (const char*)P.win; first = (b - 125) * 148; count = 41472; }
        else { base = (const char*)P.wqkv; first = (b - 406) * 148; count = 15552; }
        const int ln = first + t < count ? first + t : 0;
        touch(lds0, base + (size_t)ln * 128);
    }
    if (blockIdx.x < 256) role_g(P, tag, lds, lds0, blockIdx.x, w, lane);
    else role_a(P, tag, lds, lds0, blockIdx.x - 256, w, lane);
}

extern "C" void kernel_launch(void* const* d_in, const int* in_sizes, int n_in, void* d_out, int out_size, void* d_ws, size_t ws_size, hipStream_t stream) {
    Params P;
    P.ax = (const u16*)d_in[0]; P.wqkv = (const u16*)d_in[1]; P.wo = (const u16*)d_in[2]; P.win = (const u16*)d_in[4];
    P.wsp = (const u16*)d_in[5]; P.bsp = (const u16*)d_in[6]; P.wout = (const u16*)d_in[7]; P.gw = (const u16*)d_in[8];
    P.out = (u16*)d_out; P.ws = (char*)d_ws;
    (void)hipFuncSetAttribute((const void*)gated_mix_kernel, hipFuncAttributeMaxDynamicSharedMemorySize, LDS_BYTES);
    gated_mix_kernel<<<512, 256, LDS_BYTES, stream>>>(P);
}
```
